```python
import torch

def _softmax(x, axis=-1):
    return torch.softmax(x, dim=axis)
def _gelu(x):
    return torch.nn.functional.gelu(x, approximate="tanh")
def _silu(x):
    return torch.nn.functional.silu(x)
def _sigmoid(x):
    return torch.sigmoid(x)
def _layernorm(x, axis=-1, eps=1e-5):
    m = x.mean(dim=axis, keepdim=True)
    v = x.var(dim=axis, keepdim=True, unbiased=False)
    return (x - m) / torch.sqrt(v + eps)
def _scatter_add(tgt, idx, src, axis=0):
    return tgt.clone().index_add_(axis, idx, src)
def _segment_sum(src, seg, n):
    out = torch.zeros((n,) + src.shape[1:], dtype=src.dtype)
    return out.index_add_(0, seg, src)
def _pad(x, pw):
    flat = tuple(v for pair in reversed(pw) for v in pair)
    return torch.nn.functional.pad(x, flat)

kernel_name = 'gated_mix__sliding_local_dilated__dense_activation_mish__prompt'

def setup_inputs():

    _w = (lambda a: (lambda t: t.to(torch.bfloat16) if t.is_floating_point() else t)(torch.as_tensor(a)))
    def _A_setup():
        B, S, D = 1, 2048, 640
        return {
            "x": _w((lambda *s: torch.randn(*s, dtype=torch.bfloat16) * 0.05)(B, S, D)),
            "wqkv": _w((lambda *s: torch.randn(*s, dtype=torch.bfloat16) * 0.8)(D, 3 * D)),
            "wo": _w((lambda *s: torch.randn(*s, dtype=torch.bfloat16) * 0.05)(D, D)),
        }
    _A_in = _A_setup()

    def _B_setup():
        B, S, D, F = 1, 2048, 640, 1728
        return {
            "x": _w((lambda *s: torch.randn(*s, dtype=torch.bfloat16) * 0.05)(B, S, D)),
            "w1": _w((lambda *s: torch.randn(*s, dtype=torch.bfloat16) * 12.8)(D, F) * (D ** -0.5)),
            "b1": _w((lambda *s: torch.randn(*s, dtype=torch.bfloat16) * 0.8)(F)),
            "w2": _w((lambda *s: torch.randn(*s, dtype=torch.bfloat16) * 0.8)(F, D) * (F ** -0.5)),
        }
    _B_in = _B_setup()

    return {
        "A_x": _A_in["x"],
        "A_wqkv": _A_in["wqkv"],
        "A_wo": _A_in["wo"],
        "B_x": _B_in["x"],
        "B_w1": _B_in["w1"],
        "B_b1": _B_in["b1"],
        "B_w2": _B_in["w2"],
        "gate_w": _w((lambda *s: torch.randn(*s, dtype=torch.bfloat16) * 0.05)(640) * 16.0),
    }

def reference(A_x, A_wqkv, A_wo, B_x, B_w1, B_b1, B_w2, gate_w):
    def _A_ref(x, wqkv, wo):
        B, S, D = x.shape
        H = 32; Dh = D // H
        W, dil = 8, 2
        qkv = (lambda x, *d: x.permute(*d))((x @ wqkv).reshape(B, S, 3, H, Dh), 2, 0, 3, 1, 4)
        q, k, v = qkv[0], qkv[1], qkv[2]
        scale = Dh ** -0.5
        pos = torch.arange(S)
        off = torch.arange(W) * dil
        idx = pos[:, None] - off[None, :]
        mask = idx >= 0
        idx_c = torch.where(mask, idx, 0).reshape(-1)
        kg = k[:, :, idx_c, :].reshape(B, H, S, W, Dh)
        vg = v[:, :, idx_c, :].reshape(B, H, S, W, Dh)
        logits = torch.einsum("bhsd,bhswd->bhsw", q, kg) * scale
        logits = torch.where(mask[None, None, :, :], logits, -1e9)
        attn = _softmax(logits, axis=-1)
        o = torch.einsum("bhsw,bhswd->bhsd", attn, vg)
        return (lambda x, *d: x.permute(*d))(o, 0, 2, 1, 3).reshape(B, S, D) @ wo

    def _B_ref(x, w1, b1, w2):
        h = torch.einsum("bsd,df->bsf", x, w1) + b1
        sp = torch.log1p(torch.exp(-torch.abs(h))) + torch.where(h > 0, h, torch.zeros_like(h))
        act = h * torch.tanh(sp)
        return torch.einsum("bsf,fd->bsd", act, w2)

    x = A_x
    g = _sigmoid((x * gate_w).sum(-1, keepdims=True))
    a = _A_ref(x, A_wqkv, A_wo)
    b = _B_ref(x, B_w1, B_b1, B_w2)
    return g * a + (1 - g) * b
```

```cpp
#include <hip/hip_runtime.h>
#include <hip/hip_bf16.h>
#include <stdint.h>

typedef unsigned short u16;
using bf16x8 = __attribute__((ext_vector_type(8))) short;
using f32x4 = __attribute__((ext_vector_type(4))) float;

static __device__ __forceinline__ float bf2f(u16 v) { return __uint_as_float(((unsigned)v) << 16); }
static __device__ __forceinline__ u16 f2bf(float f) {
  __hip_bfloat16 h = __float2bfloat16(f);
  return *reinterpret_cast<u16*>(&h);
}

constexpr int S = 2048, D = 640, F = 1728, H = 32, DH = 20;
constexpr int K2 = D + F;

__global__ void gate_kernel(const u16* x, const u16* gw, float* g) {
  int row = blockIdx.x * 4 + (threadIdx.x >> 6);
  int lane = threadIdx.x & 63;
  float s = 0.f;
  for (int i = lane; i < D; i += 64) s += bf2f(x[row * D + i]) * bf2f(gw[i]);
  for (int o = 32; o > 0; o >>= 1) s += __shfl_xor(s, o);
  if (lane == 0) g[row] = 1.f / (1.f + __expf(-s));
}

template <int MODE>
__global__ __launch_bounds__(256) void gemm_kernel(const u16* A, int lda, const u16* B, int ldb, u16* C, int ldc,
                                                   int K, const u16* bias, const float* g, const u16* B2, int K1) {
  __shared__ u16 sA[64][40];
  __shared__ u16 sB[64][40];
  int bm = blockIdx.y * 64, bn = blockIdx.x * 64;
  int tid = threadIdx.x, wid = tid >> 6, lane = tid & 63;
  int wr = wid >> 1, wc = wid & 1;
  f32x4 acc[2][2] = {};
  for (int k0 = 0; k0 < K; k0 += 32) {
    {
      int r = tid >> 2, c = (tid & 3) * 8;
      *(uint4*)&sA[r][c] = *(const uint4*)&A[(size_t)(bm + r) * lda + k0 + c];
      int kr = tid >> 3, nc = (tid & 7) * 8;
      const u16* Bp = (B2 != nullptr && k0 >= K1) ? (B2 + (size_t)(k0 - K1 + kr) * ldb) : (B + (size_t)(k0 + kr) * ldb);
      uint4 v = *(const uint4*)&Bp[bn + nc];
      const u16* pv = (const u16*)&v;
      for (int j = 0; j < 8; ++j) sB[nc + j][kr] = pv[j];
    }
    __syncthreads();
    bf16x8 a[2], b[2];
    for (int m = 0; m < 2; ++m) a[m] = *(const bf16x8*)&sA[wr * 32 + m * 16 + (lane & 15)][(lane >> 4) * 8];
    for (int n = 0; n < 2; ++n) b[n] = *(const bf16x8*)&sB[wc * 32 + n * 16 + (lane & 15)][(lane >> 4) * 8];
    for (int m = 0; m < 2; ++m)
      for (int n = 0; n < 2; ++n) acc[m][n] = __builtin_amdgcn_mfma_f32_16x16x32_bf16(a[m], b[n], acc[m][n], 0, 0, 0);
    __syncthreads();
  }
  for (int m = 0; m < 2; ++m)
    for (int n = 0; n < 2; ++n)
      for (int j = 0; j < 4; ++j) {
        int row = bm + wr * 32 + m * 16 + (lane >> 4) * 4 + j;
        int col = bn + wc * 32 + n * 16 + (lane & 15);
        float v = acc[m][n][j];
        if (MODE == 1) {
          float h = v + bf2f(bias[col]);
          float e = __expf(-fabsf(h));
          float t;
          if (h > 0.f) { float nn = 1.f + 2.f * e; t = nn / (nn + 2.f * e * e); }
          else { float nn = e * (e + 2.f); t = nn / (nn + 2.f); }
          v = h * t * (1.f - g[row]);
        }
        C[(size_t)row * ldc + col] = f2bf(v);
      }
}

__global__ void attn_kernel(const u16* qkv, const float* g, u16* o, int ldo) {
  int idx = blockIdx.x * blockDim.x + threadIdx.x;
  int s = idx / H, h = idx % H;
  float q[DH];
  for (int d = 0; d < DH; ++d) q[d] = bf2f(qkv[(size_t)s * 3 * D + h * DH + d]);
  float l[8];
  float mx = -INFINITY;
  for (int w = 0; w < 8; ++w) {
    int t = s - 2 * w;
    float a = 0.f;
    if (t >= 0) {
      for (int d = 0; d < DH; ++d) a += q[d] * bf2f(qkv[(size_t)t * 3 * D + D + h * DH + d]);
      a *= 0.22360679774997896f;
    } else a = -1e9f;
    l[w] = a; mx = fmaxf(mx, a);
  }
  float sum = 0.f;
  for (int w = 0; w < 8; ++w) { l[w] = __expf(l[w] - mx); sum += l[w]; }
  float inv = g[s] / sum;
  for (int d = 0; d < DH; ++d) {
    float a = 0.f;
    for (int w = 0; w < 8; ++w) {
      int t = s - 2 * w; if (t < 0) t = 0;
      a += l[w] * bf2f(qkv[(size_t)t * 3 * D + 2 * D + h * DH + d]);
    }
    o[(size_t)s * ldo + h * DH + d] = f2bf(a * inv);
  }
}

extern "C" void kernel_launch(void* const* d_in, const int* in_sizes, int n_in, void* d_out, int out_size, void* d_ws,
                              size_t ws_size, hipStream_t stream) {
  const u16* x = (const u16*)d_in[0];
  const u16* wqkv = (const u16*)d_in[1];
  const u16* wo = (const u16*)d_in[2];
  const u16* w1 = (const u16*)d_in[4];
  const u16* b1 = (const u16*)d_in[5];
  const u16* w2 = (const u16*)d_in[6];
  const u16* gw = (const u16*)d_in[7];
  u16* out = (u16*)d_out;
  char* ws = (char*)d_ws;
  float* g = (float*)ws;
  u16* qkv = (u16*)(ws + 65536);
  u16* mid = (u16*)(ws + 65536 + (size_t)S * 3 * D * 2);
  gate_kernel<<<S / 4, 256, 0, stream>>>(x, gw, g);
  gemm_kernel<0><<<dim3(3 * D / 64, S / 64), 256, 0, stream>>>(x, D, wqkv, 3 * D, qkv, 3 * D, D, nullptr, nullptr, nullptr, 0);
  gemm_kernel<1><<<dim3(F / 64, S / 64), 256, 0, stream>>>(x, D, w1, F, mid + D, K2, D, b1, g, nullptr, 0);
  attn_kernel<<<S * H / 256, 256, 0, stream>>>(qkv, g, mid, K2);
  gemm_kernel<0><<<dim3(D / 64, S / 64), 256, 0, stream>>>(mid, K2, wo, D, out, D, K2, nullptr, nullptr, w2, D);
}
```

```cpp
#include <hip/hip_runtime.h>
#include <stdint.h>
#include <type_traits>

typedef unsigned short u16;
typedef unsigned long long u64;
typedef short bf16x8 __attribute__((ext_vector_type(8)));
typedef short s16x4 __attribute__((ext_vector_type(4)));
typedef float f32x4 __attribute__((ext_vector_type(4)));
typedef float f32x16 __attribute__((ext_vector_type(16)));
typedef float f32x2 __attribute__((ext_vector_type(2)));
typedef unsigned u32x4 __attribute__((ext_vector_type(4)));
typedef unsigned u32x2 __attribute__((ext_vector_type(2)));
typedef unsigned u32x16 __attribute__((ext_vector_type(16)));
typedef __bf16 bf16x2_t __attribute__((ext_vector_type(2)));
#define LAS __attribute__((address_space(3)))
#define GAS __attribute__((address_space(1)))
#define DI __device__ __forceinline__

#define MFMA16(a, b, c) __builtin_amdgcn_mfma_f32_16x16x32_bf16((a), (b), (c), 0, 0, 0)
#define MFMA32(a, b, c) __builtin_amdgcn_mfma_f32_32x32x16_bf16((a), (b), (c), 0, 0, 0)
#define WAIT_VM(n) asm volatile("s_waitcnt vmcnt(" #n ")" ::: "memory")
#define WAIT_VMI(n) asm volatile("s_waitcnt vmcnt(%0)" ::"i"(n) : "memory")
#define BARRIER() do { asm volatile("s_waitcnt lgkmcnt(0)" ::: "memory"); __builtin_amdgcn_s_barrier(); asm volatile("" ::: "memory"); } while (0)
#define BARRIER_RAW() do { asm volatile("" ::: "memory"); __builtin_amdgcn_s_barrier(); asm volatile("" ::: "memory"); } while (0)
#define SCHED() __builtin_amdgcn_sched_barrier(0)
#define RLX_AGENT __ATOMIC_RELAXED, __HIP_MEMORY_SCOPE_AGENT

#define TSARG
#define TSPASS
#define TSDECL
#define ST(id)
#define TSOUT()

constexpr int NTHREADS = 512;
constexpr int NKAP = 148;
constexpr int W_OFF = 18432;
constexpr int K1_STAGE = 51200;
constexpr unsigned MID_BYTES = NKAP * 64 * 1024;
constexpr unsigned GATE_OFF = MID_BYTES, FLAG_OFF = GATE_OFF + 8192, EPOCH_OFF = FLAG_OFF + 2048, WS_BYTES = EPOCH_OFF + 128;
constexpr int LDS_BYTES = 163840;
constexpr int W_SLOT = 8192;
template <int NM> struct Ring {
  static constexpr int D = NM == 3 ? 8 : 10;
  static constexpr int A_SLOT = NM * 4096;
  static constexpr int W_RING = D * A_SLOT;
};
constexpr int NST = NKAP / 4;
constexpr int SPIN_CAP = 1 << 22;

DI unsigned pack_bf16(float lo, float hi) {
  f32x2 f = {lo, hi};
  return __builtin_bit_cast(unsigned, __builtin_convertvector(f, bf16x2_t));
}
DI float bf_lo(unsigned u) { return __uint_as_float(u << 16); }
DI float bf_hi(unsigned u) { return __uint_as_float(u & 0xffff0000u); }
DI void glds16(const char* sbase, unsigned voff, unsigned dst) {
  asm volatile("s_mov_b32 m0, %2\n\ts_nop 0\n\tglobal_load_lds_dwordx4 %0, %1" : : "v"(voff), "s"(sbase), "s"(dst) : "memory");
}
DI void glds16_sc1(const char* sbase, unsigned voff, unsigned dst) {
  asm volatile("s_mov_b32 m0, %2\n\ts_nop 0\n\tglobal_load_lds_dwordx4 %0, %1 sc1" : : "v"(voff), "s"(sbase), "s"(dst) : "memory");
}
DI void glds16_lo(const char* sbase, unsigned voff, unsigned dst) {
  asm volatile("s_mov_b32 m0, %2\n\ts_mov_b32 exec_hi, 0\n\tglobal_load_lds_dwordx4 %0, %1\n\ts_mov_b32 exec_hi, -1" : : "v"(voff), "s"(sbase), "s"(dst) : "memory");
}
DI bf16x8 lds_b128(unsigned a) { return *(LAS bf16x8*)(size_t)a; }
DI s16x4 lds_tr(unsigned a) { return __builtin_amdgcn_ds_read_tr16_b64_v4i16((LAS s16x4*)(size_t)a); }
DI u32x2 lds_b64(unsigned a) { return *(LAS u32x2*)(size_t)a; }
DI void lds_w64(unsigned a, u32x2 v) { *(LAS u32x2*)(size_t)a = v; }
DI bf16x8 cat(s16x4 lo, s16x4 hi) { return __builtin_shufflevector(lo, hi, 0, 1, 2, 3, 4, 5, 6, 7); }
DI bf16x8 mk8(u32x2 a, u32x2 b) {
  u32x4 t = {a[0], a[1], b[0], b[1]};
  return __builtin_bit_cast(bf16x8, t);
}
typedef __amdgpu_buffer_rsrc_t rsrc_t;
DI void st128_sc1(rsrc_t r, unsigned off, u32x4 v) { __builtin_amdgcn_raw_buffer_store_b128(v, r, off, 0, 16); }
DI void st64_sc1(rsrc_t r, unsigned off, u32x2 v) { __builtin_amdgcn_raw_buffer_store_b64(v, r, off, 0, 16); }

DI u32x4 mish8(const f32x16& v, int o) {
  f32x2 h[4], z[4], e[4], p[4], d[4], r[4];
  const f32x2 c1 = {1.44269504088896f / 160.f, 1.44269504088896f / 160.f}, c0 = {129.f / 160.f, 129.f / 160.f};
#pragma unroll
  for (int k = 0; k < 4; ++k) { h[k] = f32x2{v[o + 2 * k], v[o + 2 * k + 1]}; asm("v_pk_fma_f32 %0, %1, %2, %3 clamp" : "=v"(z[k]) : "v"(h[k]), "v"(c1), "v"(c0)); }
#pragma unroll
  for (int k = 0; k < 4; ++k) z[k] = z[k] * 160.f - 129.f;
  SCHED();
#pragma unroll
  for (int k = 0; k < 4; ++k) e[k] = f32x2{__builtin_amdgcn_exp2f(z[k][0]), __builtin_amdgcn_exp2f(z[k][1])};
  SCHED();
#pragma unroll
  for (int k = 0; k < 4; ++k) p[k] = e[k] + 2.f;
#pragma unroll
  for (int k = 0; k < 4; ++k) d[k] = e[k] * p[k] + 2.f;
  SCHED();
#pragma unroll
  for (int k = 0; k < 4; ++k) r[k] = f32x2{__builtin_amdgcn_rcpf(d[k][0]), __builtin_amdgcn_rcpf(d[k][1])};
  SCHED();
#pragma unroll
  for (int k = 0; k < 4; ++k) h[k] = h[k] * e[k];
#pragma unroll
  for (int k = 0; k < 4; ++k) h[k] = h[k] * p[k];
#pragma unroll
  for (int k = 0; k < 4; ++k) h[k] = h[k] * r[k];
  u32x4 o4;
#pragma unroll
  for (int k = 0; k < 4; ++k) o4[k] = pack_bf16(h[k][0], h[k][1]);
  SCHED();
  return o4;
}
DI float xg_max(float v) {
  unsigned u = __float_as_uint(v);
  auto r = __builtin_amdgcn_permlane32_swap(u, u, false, false);
  v = fmaxf(__uint_as_float(r[0]), __uint_as_float(r[1]));
  u = __float_as_uint(v);
  auto r2 = __builtin_amdgcn_permlane16_swap(u, u, false, false);
  return fmaxf(__uint_as_float(r2[0]), __uint_as_float(r2[1]));
}
DI float xg_sum(float v) {
  unsigned u = __float_as_uint(v);
  auto r = __builtin_amdgcn_permlane32_swap(u, u, false, false);
  v = __uint_as_float(r[0]) + __uint_as_float(r[1]);
  u = __float_as_uint(v);
  auto r2 = __builtin_amdgcn_permlane16_swap(u, u, false, false);
  return __uint_as_float(r2[0]) + __uint_as_float(r2[1]);
}

template <int NM, int D>
constexpr int p2_allowed(int t) {
  int n = 0;
  for (int st = t + 2; st <= t + D - 2 && st < NST; ++st) n += st < D - 1 ? NM : NM + 2;
  return n;
}
template <int I, int N, class F>
DI void static_for(F&& f) {
  if constexpr (I < N) { f(std::integral_constant<int, I>{}); static_for<I + 1, N>(f); }
}
struct WLoad { unsigned voffn, voffe; const char* src; };
template <int NM>
DI WLoad p2_w_prologue(unsigned smb, int l, int lane, int ct, const u16* __restrict__ w2) {
  const int r8 = lane >> 3;
  const unsigned colb = ct * 128 + (((lane & 7) ^ (((r8 >> 1) & 1) << 2)) * 16);
  WLoad s;
  s.voffn = r8 * 1280 + colb;
  s.voffe = (20 * (r8 >> 2) + (r8 & 3)) * 1280 + colb;
  s.src = (const char*)w2 + (16 * l) * 1280;
  const unsigned d = smb + Ring<NM>::W_RING + l * 2048;
#pragma unroll 1
  for (int t = 0; t < Ring<NM>::D - 1; ++t) {
    glds16(s.src, s.voffn, d + t * W_SLOT);
    glds16(s.src + 8 * 1280, s.voffn, d + t * W_SLOT + 1024);
    s.src += 64 * 1280;
  }
  return s;
}
DI void spin_flags(GAS u64* fp, u64 tag) {
#pragma unroll 1
  for (int spin = 0; spin < SPIN_CAP; ++spin) {
    if (__all(__hip_atomic_load(fp, RLX_AGENT) == tag)) break;
    __builtin_amdgcn_s_sleep(2);
  }
  asm volatile("" ::: "memory");
}
template <int NM>
DI void p2_loader(unsigned smb, int l, int lane, int m0, WLoad& ws, const char* __restrict__ mid, const u16* __restrict__ wo,
                  GAS u64* fpm, GAS u64* fpq, u64 tag, GAS u64* myflag) {
  constexpr int D = Ring<NM>::D, A_SLOT = Ring<NM>::A_SLOT;
  const unsigned lane16 = lane * 16;
  const char* src = mid + (size_t)((40 + l) * 64 + m0) * 1024;
  const unsigned da = smb + l * (NM * 1024), dw = smb + Ring<NM>::W_RING + l * 2048;
  auto issue_a = [&](unsigned slot) {
#pragma unroll
    for (int m = 0; m < NM; ++m) glds16_sc1(src + m * 1024, lane16, da + slot * A_SLOT + m * 1024);
    src += 4 * 65536;
  };
  spin_flags(fpm, tag);
#pragma unroll 1
  for (int t = 0; t < D - 1; ++t) issue_a(t);
  WAIT_VMI(NM * (D - 2));
  BARRIER_RAW();
  if (NM == 2 && l == 2 && lane == 0) {
    asm volatile("global_store_dwordx2 %0, %1, off sc1" : : "v"(myflag), "v"(tag) : "memory");
  }
  unsigned ps = D - 1;
  u64 fq = 0;
  auto issue_act = [&]() {
    glds16(ws.src, ws.voffn, dw + ps * W_SLOT);
    glds16(ws.src + 8 * 1280, ws.voffn, dw + ps * W_SLOT + 1024);
    ws.src += 64 * 1280;
    issue_a(ps);
    ps = (ps == D - 1) ? 0 : ps + 1;
  };
  static_for<0, D - 3>([&](auto tc) { WAIT_VMI((p2_allowed<NM, D>(tc.value))); BARRIER_RAW(); issue_act(); });
#pragma unroll 1
  for (int t = D - 3; t + D - 1 <= 24; ++t) { WAIT_VMI((NM + 2) * (D - 3)); BARRIER_RAW(); issue_act(); }
  asm volatile("global_load_dwordx2 %0, %1, off sc1" : "=v"(fq) : "v"(fpq) : "memory");
  WAIT_VMI((NM + 2) * (D - 3)); BARRIER_RAW(); issue_act();
  WAIT_VMI((NM + 2) * (D - 3)); BARRIER_RAW(); issue_act();
  asm volatile("s_waitcnt vmcnt(%1)" : "+v"(fq) : "i"(2 * (NM + 2)) : "memory");
  if (!__all(fq == tag)) spin_flags(fpq, tag);
  src = mid + (size_t)(l * 64 + m0) * 1024;
  int u = l;
  const char* won = (const char*)wo;
#pragma unroll 1
  for (int j = 0; j < 10; ++j) {
    WAIT_VMI((NM + 2) * (D - 3)); BARRIER_RAW();
    const int r0 = u < 4 ? 40 * (u >> 1) + 8 * (u & 1) : 16, dr = u < 4 ? 20 : 40;
    const unsigned vo = u < 4 ? ws.voffn : ws.voffe;
    glds16(won + r0 * 1280, vo, dw + ps * W_SLOT);
    glds16(won + (r0 + dr) * 1280, vo, dw + ps * W_SLOT + 1024);
    won += u >= 1 ? 80 * 1280 : 0;
    u = u >= 1 ? u - 1 : 4;
    issue_a(ps);
    ps = (ps == D - 1) ? 0 : ps + 1;
  }
  static_for<NST - D + 1, NST - 1>([&](auto tc) { WAIT_VMI((p2_allowed<NM, D>(tc.value))); BARRIER_RAW(); });
}

DI void qkv_slot(int sg, int odd, int& P, int& beta) {
  if (sg < 12) { P = sg >> 2; beta = (sg & 3) + odd; }
  else { P = sg < 15 ? sg - 12 : 2; beta = odd ? 0 : 4; }
}
template <int QKV>
DI void p1_loader(unsigned smb, int L, int lane, const char* __restrict__ x, const char* __restrict__ wmat, int row0, int cg, int ps, int np) {
  constexpr unsigned RB = QKV ? 3840 : 3456;
  constexpr int NL = QKV ? 13 : 12;
  unsigned vx = (row0 + 8 * L + (lane >> 3)) * 1280 + (((lane & 7) ^ (4 * (L & 1) + (lane >> 4))) * 16);
  const int hr = 4 * L + ((lane >> 3) & 3);
  int tokh = row0 - 16 + hr; tokh = tokh < 0 ? 0 : tokh;
  unsigned vh = tokh * 1280 + (((lane & 7) ^ ((hr >> 1) & 7)) * 16);
  const int hi = lane >> 5;
  unsigned vw[2];
#pragma unroll
  for (int e = 0; e < 2; ++e) {
    const int c = (lane & 31) ^ ((((2 * L + hi) & 3) << 2) | (e << 1));
    unsigned col;
    if (QKV) { int P, beta; qkv_slot(c >> 1, cg & 1, P, beta); col = (P == 2 ? 0 : (P + 1) * 1280) + cg * 160 + beta * 32 + (c & 1) * 16; }
    else col = ps * 64 + (c < 4 * np ? c : 4 * np - 1) * 16;
    vw[e] = (2 * L + hi) * RB + col;
  }
  auto issue = [&](unsigned buf) {
    const unsigned d = smb + buf + L * 1024;
#pragma unroll
    for (int j = 0; j < 8; ++j) {
      glds16(wmat + j * (8 * RB), vw[j & 1], d + W_OFF + j * 4096);
      if (j < 4) glds16(x + j * (32 * 1280), vx, d + 2048 + j * 4096);
      if (QKV && j == 4) glds16_lo(x, vh, smb + buf + L * 512);
    }
    vx += 128; vh += 128; vw[0] += 64 * RB; vw[1] += 64 * RB;
  };
  issue(0); issue(K1_STAGE);
  unsigned nb = 2 * K1_STAGE;
#pragma unroll 1
  for (int t = -1; t < 8; ++t) {
    WAIT_VMI(NL);
    BARRIER_RAW();
    if (t <= 6) issue(nb);
    nb = (nb == 2 * K1_STAGE) ? 0 : nb + K1_STAGE;
  }
  WAIT_VM(0);
  BARRIER_RAW();
}

struct Frag { bf16x8 x[2]; s16x4 lo[4], hi[4]; };
struct HFrag { bf16x8 x; s16x4 lo[3], hi[3]; };

template <int NMM>
DI void p1_read(Frag& F, int idx, unsigned ax0, unsigned ax1, const unsigned (&aw)[4], int s) {
  if (idx == 0) F.x[0] = lds_b128(ax0);
  else if (idx == 3) F.x[1] = lds_b128(ax1);
  else if (idx < (NMM > 6 ? 10 : 8)) {
    const int k = idx < 3 ? idx - 1 : idx - 2;
    if ((k & 1) == 0) F.lo[k >> 1] = lds_tr(aw[k >> 1] + W_OFF + s * 8192);
    else F.hi[k >> 1] = lds_tr(aw[k >> 1] + W_OFF + s * 8192 + 2048);
  }
}

template <int NMM, int NH>
DI void p1_compute(f32x16 (&acc)[2][4], f32x4 (&ah)[3], unsigned smb, unsigned lx0, unsigned lx1, const unsigned (&lw)[4],
                   unsigned lxh, const unsigned (&lh)[3] TSARG) {
  Frag F[2];
  HFrag H;
  unsigned aw[4];
#pragma unroll
  for (int n = 0; n < 4; ++n) aw[n] = smb + lw[n];
  BARRIER_RAW();
  ST(1);
#pragma unroll
  for (int i = 0; i < 10; ++i) p1_read<NMM>(F[0], i, smb + lx0, smb + lx1, aw, 0);
  unsigned cur = smb;
#pragma unroll 1
  for (int t = 0; t < 10; ++t) {
    const unsigned nxt = (cur == smb + 2 * K1_STAGE) ? smb : cur + K1_STAGE;
#pragma unroll
    for (int s = 0; s < 4; ++s) {
      const Frag& C = F[s & 1];
      Frag& N = F[(s + 1) & 1];
      const unsigned nb = s == 3 ? nxt : cur;
      const int ns = (s + 1) & 3;
      const unsigned ax0 = nb + (lx0 ^ (32 * ns)), ax1 = nb + (lx1 ^ (32 * ns));
#pragma unroll
      for (int mm = 0; mm < NMM; ++mm) {
        const int n = mm >> 1, m = mm & 1;
        acc[m][n] = MFMA32(cat(C.lo[n], C.hi[n]), C.x[m], acc[m][n]);
        if (s == 3 && mm == 0) {
          SCHED();
          BARRIER();
#pragma unroll
          for (int q = 0; q < 4; ++q) aw[q] = nxt + lw[q];
        }
        p1_read<NMM>(N, 2 * mm, ax0, ax1, aw, ns); p1_read<NMM>(N, 2 * mm + 1, ax0, ax1, aw, ns);
        if (NH) {
          if (mm == 4 && (s == 1 || s == 2)) {
#pragma unroll
            for (int j = 0; j < NH; ++j) ah[j] = MFMA16(cat(H.lo[j], H.hi[j]), H.x, ah[j]);
          }
          if (mm >= 5 && s < 2) {
            if (mm == 5) H.x = lds_b128(cur + (lxh ^ (64 * s)));
            if (mm - 5 < NH) { H.lo[mm - 5] = lds_tr(cur + lh[mm - 5] + W_OFF + s * 16384); H.hi[mm - 5] = lds_tr(cur + lh[mm - 5] + W_OFF + s * 16384 + 2048); }
          }
        }
        SCHED();
      }
    }
    cur = nxt;
  }
  ST(2);
}

DI unsigned p1_lx(int m32, int lane) {
  const int r = lane & 31;
  return (16 + 32 * m32 + r) * 128 + ((((lane >> 5)) ^ ((r >> 1) & 7)) * 16);
}
DI unsigned p1_lw(int T, int lane) {
  const int h = lane >> 5, q = (lane >> 2) & 3, p = lane & 3, cb = (lane >> 4) & 1;
  return (8 * h + q) * 512 + ((64 * T + 32 * cb + 8 * p) ^ ((q << 6) | (h << 5)));
}

template <int NMM>
DI void p1_mlp(unsigned smb, int w, int lane, int f0, int rt, const u16* __restrict__ b1, rsrc_t ws TSARG) {
  const int h = lane >> 5;
  const int nh = w >> 1;
  const int ma = 2 * (w & 1) + nh, mb = ma ^ 1;
  const int T[4] = {3 * nh, 3 * nh + 1, 3 * nh + 2, 6};
  f32x16 acc[2][4]; f32x4 ah[3];
  unsigned lw[4];
  u32x2 bv[4][4];
#pragma unroll
  for (int n = 0; n < 4; ++n)
#pragma unroll
    for (int a = 0; a < 4; ++a)
      if (n < 3 || NMM > 6) bv[n][a] = *(const u32x2*)(b1 + f0 + 32 * T[n] + 8 * a + 4 * h);
  SCHED();
#pragma unroll
  for (int n = 0; n < 4; ++n) {
    lw[n] = p1_lw(T[n], lane);
    if (n < 3 || NMM > 6) {
#pragma unroll
      for (int a = 0; a < 4; ++a) {
        acc[0][n][4 * a] = bf_lo(bv[n][a][0]); acc[0][n][4 * a + 1] = bf_hi(bv[n][a][0]);
        acc[0][n][4 * a + 2] = bf_lo(bv[n][a][1]); acc[0][n][4 * a + 3] = bf_hi(bv[n][a][1]);
      }
      acc[1][n] = acc[0][n];
      asm volatile("" : "+v"(acc[0][n]), "+v"(acc[1][n]));
    }
  }
  const unsigned lh[3] = {0u, 0u, 0u};
  p1_compute<NMM, 0>(acc, ah, smb, p1_lx(ma, lane), p1_lx(mb, lane), lw, 0u, lh TSPASS);
  asm volatile("s_nop 15\n\ts_nop 15" : "+v"(acc[0][0]), "+v"(acc[1][0]), "+v"(acc[0][1]), "+v"(acc[1][1]), "+v"(acc[0][2]), "+v"(acc[1][2]));
  if (NMM > 6) asm volatile("" : "+v"(acc[0][3]));
#pragma unroll
  for (int mm = 0; mm < NMM; ++mm) {
    const int n = mm >> 1, m = mm & 1;
    const unsigned dst = ((40 + (f0 >> 4) + 2 * T[n]) * 64 + rt * 4 + (m ? mb : ma)) * 1024 + lane * 16;
#pragma unroll
    for (int u = 0; u < 2; ++u) {
      const u32x4 o = mish8(acc[m][n], 8 * u);
      st128_sc1(ws, dst + u * 65536, o);
    }
  }
}

constexpr int XMT = 7680, XDUMP = 9 * XMT;
DI unsigned xslot_off(int sg, int odd) {
  int part, beta; qkv_slot(sg, odd, part, beta);
  return part * 2560 + beta * 512;
}

template <int NH>
DI void p1_qkv(unsigned smb, int w, int lane, int odd TSARG) {
  const int h = lane >> 5, g = lane >> 4, i = lane & 15;
  const int ma = 2 * (w & 1), T0 = 4 * (w >> 1);
  f32x16 acc[2][4] = {}; f32x4 ah[3] = {};
  const unsigned lxh = i * 128 + ((g ^ (i >> 1)) * 16);
  const int q = (lane >> 2) & 3, p = lane & 3;
  const unsigned hrow = (8 * g + q) * 512, hsw = (q << 6) | ((g & 1) << 5);
  const int hs[3] = {2 * w, 2 * w + 1, 12 + (w & 1)};
  unsigned lh[3], lw[4];
#pragma unroll
  for (int j = 0; j < 3; ++j) lh[j] = hrow + ((32 * hs[j] + 8 * p) ^ hsw);
#pragma unroll
  for (int n = 0; n < 4; ++n) { lw[n] = p1_lw(T0 + n, lane); asm volatile("" : "+v"(acc[0][n]), "+v"(acc[1][n])); }
  p1_compute<8, NH>(acc, ah, smb, p1_lx(ma, lane), p1_lx(ma + 1, lane), lw, lxh, lh TSPASS);
  const unsigned lb = smb + (1 + 2 * ma + (g & 1)) * XMT + h * 128 + i * 8;
#pragma unroll
  for (int n = 0; n < 4; ++n)
#pragma unroll
    for (int a2 = 0; a2 < 2; ++a2) {
      const int sg = 2 * (T0 + n) + a2;
      if (n == 3 && a2 == 1 && sg == 15) continue;
      const unsigned xo = lb + xslot_off(sg, odd);
#pragma unroll
      for (int a1 = 0; a1 < 2; ++a1)
#pragma unroll
        for (int m = 0; m < 2; ++m) {
          const int a = 2 * a2 + a1;
          lds_w64(xo + a1 * 256 + m * 2 * XMT, u32x2{pack_bf16(acc[m][n][4 * a], acc[m][n][4 * a + 1]), pack_bf16(acc[m][n][4 * a + 2], acc[m][n][4 * a + 3])});
        }
    }
#pragma unroll
  for (int j = 0; j < NH; ++j)
    lds_w64(smb + xslot_off(hs[j], odd) + lane * 8, u32x2{pack_bf16(ah[j][0], ah[j][1]), pack_bf16(ah[j][2], ah[j][3])});
}

DI void p1_attn(unsigned smb, int w, int lane, int rt, int hg, rsrc_t ws) {
  const int g = lane >> 4, i = lane & 15;
  const unsigned base = smb + w * XMT;
  const unsigned pa = base + lane * 8, pe = base + (5 * g + 4) * 128 + i * 8;
  const unsigned va = base + 2560 + (lane & 3) * 128 + (lane >> 2) * 8, ve = base + 2560 + (5 * (lane & 3) + 4) * 128 + (lane >> 2) * 8;
  u32x2 q[5], kp[5], kc[5], vp[5], vc[5];
#pragma unroll
  for (int t = 0; t < 5; ++t) {
    const unsigned a = t < 4 ? pa + t * 640 : pe;
    q[t] = lds_b64(a + XMT + 5120);
    kp[t] = lds_b64(a); kc[t] = lds_b64(a + XMT);
  }
#pragma unroll
  for (int t = 0; t < 5; ++t) {
    const unsigned a = t < 4 ? va + t * 640 : ve;
    vp[t] = __builtin_bit_cast(u32x2, lds_tr(a)); vc[t] = __builtin_bit_cast(u32x2, lds_tr(a + XMT));
  }
  const int par = i & 1;
  const bool c0 = (4 * g + par) <= i, c1 = (4 * g + par + 2) <= i;
  const bool first = (rt == 0) && (w == 0);
  const unsigned hm = par ? 0xffff0000u : 0x0000ffffu;
  const unsigned m_p0 = c0 ? 0u : hm, m_c0 = c0 ? hm : 0u, m_p1 = c1 ? 0u : hm, m_c1 = c1 ? hm : 0u;
  const float cs = 0.22360679774997896f * 1.44269504088896f;
  const f32x4 z4 = {0.f, 0.f, 0.f, 0.f};
  f32x4 oe = z4;
  float inv_e = 0.f;
  u32x2 op[4];
#pragma unroll
  for (int j = 0; j < 4; ++j) {
    u32x2 qe = q[4]; if (g != j) qe = u32x2{0u, 0u};
    bf16x8 qop = mk8(q[j], qe);
    f32x4 sp = MFMA16(mk8(kp[j], kp[4]), qop, z4);
    f32x4 sc = MFMA16(mk8(kc[j], kc[4]), qop, z4);
    float a0 = par ? sc[1] : sc[0], a1 = par ? sc[3] : sc[2];
    float b0 = par ? sp[1] : sp[0], b1 = par ? sp[3] : sp[2];
    if (first) { b0 = -INFINITY; b1 = -INFINITY; }
    float s0 = c0 ? a0 : b0, s1 = c1 ? a1 : b1;
    float mx = xg_max(fmaxf(s0, s1));
    float nm = -mx * cs;
    float p0 = __builtin_amdgcn_exp2f(fmaf(s0, cs, nm)), p1 = __builtin_amdgcn_exp2f(fmaf(s1, cs, nm));
    unsigned w0 = pack_bf16(p0, p0), w1 = pack_bf16(p1, p1);
    float sum = xg_sum(bf_lo(w0) + bf_lo(w1));
    float inv = __builtin_amdgcn_rcpf(sum);
    u32x4 pw = {w0 & m_p0, w1 & m_p1, w0 & m_c0, w1 & m_c1};
    bf16x8 pop = __builtin_bit_cast(bf16x8, pw);
    f32x4 o = MFMA16(mk8(vp[j], vc[j]), pop, z4);
    u32x2 ep = vp[4], ec = vc[4];
    if ((i >> 2) != j) { ep = u32x2{0u, 0u}; ec = u32x2{0u, 0u}; }
    oe = MFMA16(mk8(ep, ec), pop, oe);
    if (g == j) inv_e = inv;
    op[j] = u32x2{pack_bf16(o[0] * inv, o[1] * inv), pack_bf16(o[2] * inv, o[3] * inv)};
  }
  const int m32 = rt * 4 + (w >> 1);
  const unsigned lp = (32 * (g & 1) + 16 * (w & 1) + i) * 16;
  const unsigned d0 = ((5 * hg + (g >> 1)) * 64 + m32) * 1024 + lp;
  st128_sc1(ws, d0, u32x4{op[0][0], op[0][1], op[1][0], op[1][1]});
  st128_sc1(ws, d0 + 2 * 65536, u32x4{op[2][0], op[2][1], op[3][0], op[3][1]});
  st64_sc1(ws, ((5 * hg + 4) * 64 + m32) * 1024 + lp + (g >> 1) * 8,
           u32x2{pack_bf16(oe[0] * inv_e, oe[1] * inv_e), pack_bf16(oe[2] * inv_e, oe[3] * inv_e)});
}

template <int NM> struct Frag2 { bf16x8 a[2][NM]; s16x4 lo[2], hi[2]; };
template <int NM>
DI void p2_stage(const Frag2<NM>& C, Frag2<NM>& N, unsigned pa, unsigned pw, f32x16 (&acc)[NM]) {
#pragma unroll
  for (int k = 0; k < 2; ++k) {
#pragma unroll
    for (int m = 0; m < NM; ++m) {
      acc[m] = MFMA32(cat(C.lo[k], C.hi[k]), C.a[k][m], acc[m]);
      if (m == 0) { N.lo[k] = lds_tr(pw + k * 2048); N.hi[k] = lds_tr(pw + k * 2048 + 1024); }
      N.a[k][m] = lds_b128(pa + (k * NM + m) * 1024);
      SCHED();
    }
  }
}
constexpr int RED_OFF = 16384;
template <int NM>
DI void p2_compute(unsigned smb, int w, int lane, int m0, GAS float* gate TSARG) {
  const int kh = w >> 1, nh = w & 1, h = lane >> 5;
  constexpr int D = Ring<NM>::D, A_SLOT = Ring<NM>::A_SLOT;
  const unsigned la = smb + kh * (2 * NM * 1024) + lane * 16;
  const int q = (lane >> 2) & 3;
  const unsigned lw = smb + Ring<NM>::W_RING + kh * 4096 + (4 * h + q) * 128 + ((64 * nh + 32 * ((lane >> 4) & 1) + 8 * (lane & 3)) ^ ((q >> 1) << 6));
  f32x16 accA[NM] = {}, accB[NM] = {};
  Frag2<NM> F0, F1;
  WAIT_VM(0);
  BARRIER_RAW();
  ST(7);
  float gv[NM];
#pragma unroll
  for (int m = 0; m < NM; ++m) gv[m] = __hip_atomic_load(gate + (m0 + m) * 32 + (lane & 31), RLX_AGENT);
#pragma unroll
  for (int k = 0; k < 2; ++k) {
    F0.lo[k] = lds_tr(lw + k * 2048); F0.hi[k] = lds_tr(lw + k * 2048 + 1024);
#pragma unroll
    for (int m = 0; m < NM; ++m) F0.a[k][m] = lds_b128(la + (k * NM + m) * 1024);
  }
  unsigned ca = A_SLOT, cw = W_SLOT;
  auto adv = [&]() { ca = (ca == (D - 1) * A_SLOT) ? 0 : ca + A_SLOT; cw = (cw == (D - 1) * W_SLOT) ? 0 : cw + W_SLOT; };
#pragma unroll 1
  for (int t = 0; t < 26; t += 2) {
    BARRIER_RAW(); p2_stage<NM>(F0, F1, la + ca, lw + cw, accB); adv();
    BARRIER_RAW(); p2_stage<NM>(F1, F0, la + ca, lw + cw, accB); adv();
  }
  BARRIER_RAW(); p2_stage<NM>(F0, F1, la + ca, lw + cw, accB); adv();
  ST(8);
  BARRIER_RAW(); p2_stage<NM>(F1, F0, la + ca, lw + cw, accA); adv();
#pragma unroll 1
  for (int t = 28; t < NST - 1; t += 2) {
    BARRIER_RAW(); p2_stage<NM>(F0, F1, la + ca, lw + cw, accA); adv();
    BARRIER_RAW(); p2_stage<NM>(F1, F0, la + ca, lw + cw, accA); adv();
  }
#pragma unroll
  for (int k = 0; k < 2; ++k)
#pragma unroll
    for (int m = 0; m < NM; ++m) accA[m] = MFMA32(cat(F0.lo[k], F0.hi[k]), F0.a[k][m], accA[m]);
  ST(9);
  BARRIER();
  f32x4 keep[NM][2];
#pragma unroll
  for (int m = 0; m < NM; ++m) {
    const float hv = 1.f - gv[m];
#pragma unroll
    for (int a = 0; a < 2; ++a) {
      f32x4 lo, hi;
#pragma unroll
      for (int e = 0; e < 4; ++e) {
        lo[e] = gv[m] * accA[m][4 * a + e] + hv * accB[m][4 * a + e];
        hi[e] = gv[m] * accA[m][8 + 4 * a + e] + hv * accB[m][8 + 4 * a + e];
      }
      keep[m][a] = kh ? hi : lo;
      *(LAS f32x4*)(size_t)(smb + RED_OFF + ((w * NM + m) * 2 + a) * 1024 + lane * 16) = kh ? lo : hi;
    }
  }
  BARRIER();
#pragma unroll
  for (int m = 0; m < NM; ++m)
#pragma unroll
    for (int a = 0; a < 2; ++a) {
      const f32x4 o = keep[m][a] + *(LAS f32x4*)(size_t)(smb + RED_OFF + (((w ^ 2) * NM + m) * 2 + a) * 1024 + lane * 16);
      lds_w64(smb + (m * 32 + (lane & 31)) * 144 + nh * 64 + (2 * kh + a) * 16 + h * 8, u32x2{pack_bf16(o[0], o[1]), pack_bf16(o[2], o[3])});
    }
}

__global__ __launch_bounds__(NTHREADS, 2) void fused_kernel(const u16* __restrict__ x, const u16* __restrict__ wqkv, const u16* __restrict__ wo,
                                                            const u16* __restrict__ w1, const u16* __restrict__ b1, const u16* __restrict__ w2,
                                                            const u16* __restrict__ gw, char* __restrict__ wsp, u16* __restrict__ out) {
  extern __shared__ __attribute__((aligned(16))) char smem[];
  const unsigned smb = (unsigned)(size_t)(LAS char*)smem;
  TSDECL; ST(0);
  const int lane = threadIdx.x & 63;
  const int w = __builtin_amdgcn_readfirstlane(threadIdx.x >> 6);
  const int b = blockIdx.x, xcd = b & 7, idx = b >> 3;
  const int is_mlp = idx & 1;
  const int rt = (xcd >> 1) * 4 + ((idx >> 1) & 3);
  const int cg = (xcd & 1) * 4 + (idx >> 3);
  const int row0 = rt * 128;
  const int ps = cg < 7 ? 7 * cg : 48, np = cg < 6 ? 7 : 6;
  int ct, m0;
  {
    const int j = idx >> 1;
    if (is_mlp) { if (j < 12) { ct = j >> 2; m0 = 3 * (j & 3); } else { ct = 3 + ((j - 12) >> 1); m0 = 3 * (j & 1); } }
    else { if (j < 6) { ct = j >> 1; m0 = 12 + 2 * (j & 1); } else { const int jj = j - 6; ct = 3 + (jj >= 5); m0 = 6 + 2 * (jj >= 5 ? jj - 5 : jj); } }
    ct += (xcd & 1) * 5; m0 += (xcd >> 1) * 16;
  }
  const int nm = is_mlp ? 3 : 2;
  const rsrc_t ws = __builtin_amdgcn_make_buffer_rsrc(wsp, 0, WS_BYTES, 0x00020000);
  GAS u64* flags = (GAS u64*)(wsp + FLAG_OFF);
  GAS u64* epoch = (GAS u64*)(wsp + EPOCH_OFF);
  GAS float* gate = (GAS float*)(wsp + GATE_OFF);

  WLoad wl = {0u, 0u, nullptr};
  u64 tag = 0;
  if (w >= 4) {
    if (is_mlp) p1_loader<0>(smb, w - 4, lane, (const char*)x, (const char*)w1, row0, cg, ps, np);
    else p1_loader<1>(smb, w - 4, lane, (const char*)x, (const char*)wqkv, row0, cg, ps, np);
    wl = is_mlp ? p2_w_prologue<3>(smb, w - 4, lane, ct, w2) : p2_w_prologue<2>(smb, w - 4, lane, ct, w2);
    BARRIER_RAW();
    tag = __hip_atomic_load(epoch, RLX_AGENT) * 0x9E3779B97F4A7C15ull + 0x632BE59BD9B4E019ull;
    if (w >= 6) {
      if (is_mlp) {
        const int row = row0 + cg * 16 + (w - 6) * 8 + (lane >> 3);
        const char* xr = (const char*)x + row * 1280 + (lane & 7) * 16;
        const char* gr = (const char*)gw + (lane & 7) * 16;
        float s = 0.f;
#pragma unroll
        for (int t = 0; t < 10; ++t) {
          u32x4 a = *(const u32x4*)(xr + t * 128), c = *(const u32x4*)(gr + t * 128);
#pragma unroll
          for (int e = 0; e < 4; ++e) {
            const unsigned ua = a[e], uc = c[e];
            s = __builtin_amdgcn_fdot2_f32_bf16(__builtin_bit_cast(bf16x2_t, ua), __builtin_bit_cast(bf16x2_t, uc), s, false);
          }
        }
        s += __shfl_xor(s, 1); s += __shfl_xor(s, 2); s += __shfl_xor(s, 4);
        if ((lane & 7) == 0) __hip_atomic_store(gate + row, 1.f / (1.f + __expf(-s)), RLX_AGENT);
      }
    }
    ST(1); ST(2);
  } else if (is_mlp) {
    const int f0 = ps * 32;
    if (np == 7) p1_mlp<7>(smb, w, lane, f0, rt, b1, ws TSPASS);
    else p1_mlp<6>(smb, w, lane, f0, rt, b1, ws TSPASS);
  } else {
    if (w < 2) p1_qkv<3>(smb, w, lane, cg & 1 TSPASS);
    else p1_qkv<2>(smb, w, lane, cg & 1 TSPASS);
  }
  if (!is_mlp) {
    BARRIER();
    ST(3);
    p1_attn(smb, w, lane, rt, cg, ws);
  } else { ST(3); }
  ST(4);
  GAS u64* myflag = flags + rt * 16 + is_mlp * 8 + cg;
  if (is_mlp) WAIT_VM(0);
  BARRIER();
  ST(5);
  if (is_mlp && w == 6 && lane == 0) __hip_atomic_store(myflag, tag, RLX_AGENT);
  ST(6);
  if (w >= 4) {
    const int rta = m0 >> 2, rtb = (m0 + nm - 1) >> 2;
    GAS u64* fq = flags + ((lane & 8) ? rtb : rta) * 16 + (lane & 7);
    if (is_mlp) p2_loader<3>(smb, w - 4, lane, m0, wl, wsp, wo, fq + 8, fq, tag, myflag);
    else p2_loader<2>(smb, w - 4, lane, m0, wl, wsp, wo, fq + 8, fq, tag, myflag);
    ST(7); ST(8); ST(9);
    BARRIER_RAW();
    BARRIER_RAW();
  } else if (is_mlp) p2_compute<3>(smb, w, lane, m0, gate TSPASS);
  else p2_compute<2>(smb, w, lane, m0, gate TSPASS);
  BARRIER();
  for (int c = threadIdx.x; c < nm * 256; c += NTHREADS) {
    const int r = c >> 3, ch = c & 7;
    u32x4 v = *(LAS u32x4*)(size_t)(smb + r * 144 + ch * 16);
    *(u32x4*)((char*)out + (size_t)(m0 * 32 + r) * 1280 + ct * 128 + ch * 16) = v;
  }
  if (b == 254 && w == 7) {
    for (int spin = 0; spin < SPIN_CAP; ++spin) {
      bool ok = true;
#pragma unroll
      for (int k = 0; k < 4; ++k) ok &= __hip_atomic_load(flags + k * 64 + lane, RLX_AGENT) == tag;
      if (__all(ok)) break;
      __builtin_amdgcn_s_sleep(8);
    }
    if (lane == 0) __hip_atomic_store(epoch, tag, RLX_AGENT);
  }
}

extern "C" void kernel_launch(void* const* d_in, const int* in_sizes, int n_in, void* d_out, int out_size, void* d_ws,
                              size_t ws_size, hipStream_t stream) {
  (void)hipFuncSetAttribute((const void*)fused_kernel, hipFuncAttributeMaxDynamicSharedMemorySize, LDS_BYTES);
  fused_kernel<<<256, NTHREADS, LDS_BYTES, stream>>>((const u16*)d_in[0], (const u16*)d_in[1], (const u16*)d_in[2], (const u16*)d_in[4],
                                                     (const u16*)d_in[5], (const u16*)d_in[6], (const u16*)d_in[7], (char*)d_ws, (u16*)d_out);
}
```

```cpp
#include <hip/hip_runtime.h>
#include <stdint.h>
#include <type_traits>

typedef unsigned short u16;
typedef unsigned long long u64;
typedef short bf16x8 __attribute__((ext_vector_type(8)));
typedef short s16x4 __attribute__((ext_vector_type(4)));
typedef float f32x4 __attribute__((ext_vector_type(4)));
typedef float f32x16 __attribute__((ext_vector_type(16)));
typedef float f32x2 __attribute__((ext_vector_type(2)));
typedef unsigned u32x4 __attribute__((ext_vector_type(4)));
typedef unsigned u32x2 __attribute__((ext_vector_type(2)));
typedef unsigned u32x16 __attribute__((ext_vector_type(16)));
typedef __bf16 bf16x2_t __attribute__((ext_vector_type(2)));
#define LAS __attribute__((address_space(3)))
#define GAS __attribute__((address_space(1)))
#define DI __device__ __forceinline__

#define MFMA16(a, b, c) __builtin_amdgcn_mfma_f32_16x16x32_bf16((a), (b), (c), 0, 0, 0)
#define MFMA32(a, b, c) __builtin_amdgcn_mfma_f32_32x32x16_bf16((a), (b), (c), 0, 0, 0)
#define WAIT_VM(n) asm volatile("s_waitcnt vmcnt(" #n ")" ::: "memory")
#define WAIT_VMI(n) asm volatile("s_waitcnt vmcnt(%0)" ::"i"(n) : "memory")
#define BARRIER() do { asm volatile("s_waitcnt lgkmcnt(0)" ::: "memory"); __builtin_amdgcn_s_barrier(); asm volatile("" ::: "memory"); } while (0)
#define BARRIER_RAW() do { asm volatile("" ::: "memory"); __builtin_amdgcn_s_barrier(); asm volatile("" ::: "memory"); } while (0)
#define SCHED() __builtin_amdgcn_sched_barrier(0)
#define RLX_AGENT __ATOMIC_RELAXED, __HIP_MEMORY_SCOPE_AGENT

#define TSARG
#define TSPASS
#define TSDECL
#define ST(id)
#define TSOUT()

constexpr int NTHREADS = 512;
constexpr int NKAP = 148;
constexpr int W_OFF = 18432;
constexpr int K1_STAGE = 51200;
constexpr unsigned MID_BYTES = NKAP * 64 * 1024;
constexpr unsigned GATE_OFF = MID_BYTES, FLAG_OFF = GATE_OFF + 8192, EPOCH_OFF = FLAG_OFF + 2048, WS_BYTES = EPOCH_OFF + 128;
constexpr int LDS_BYTES = 163840;
constexpr int W_SLOT = 8192;
template <int NM> struct Ring {
  static constexpr int D = NM == 3 ? 8 : 10;
  static constexpr int A_SLOT = NM * 4096;
  static constexpr int W_RING = D * A_SLOT;
};
constexpr int NST = NKAP / 4;
constexpr int SPIN_CAP = 1 << 22;

DI unsigned pack_bf16(float lo, float hi) {
  f32x2 f = {lo, hi};
  return __builtin_bit_cast(unsigned, __builtin_convertvector(f, bf16x2_t));
}
DI float bf_lo(unsigned u) { return __uint_as_float(u << 16); }
DI float bf_hi(unsigned u) { return __uint_as_float(u & 0xffff0000u); }
DI void glds16(const char* sbase, unsigned voff, unsigned dst) {
  asm volatile("s_mov_b32 m0, %2\n\ts_nop 0\n\tglobal_load_lds_dwordx4 %0, %1" : : "v"(voff), "s"(sbase), "s"(dst) : "memory");
}
DI void glds16_sc1(const char* sbase, unsigned voff, unsigned dst) {
  asm volatile("s_mov_b32 m0, %2\n\ts_nop 0\n\tglobal_load_lds_dwordx4 %0, %1 sc1" : : "v"(voff), "s"(sbase), "s"(dst) : "memory");
}
DI void glds16_lo(const char* sbase, unsigned voff, unsigned dst) {
  asm volatile("s_mov_b32 m0, %2\n\ts_mov_b32 exec_hi, 0\n\tglobal_load_lds_dwordx4 %0, %1\n\ts_mov_b32 exec_hi, -1" : : "v"(voff), "s"(sbase), "s"(dst) : "memory");
}
DI bf16x8 lds_b128(unsigned a) { return *(LAS bf16x8*)(size_t)a; }
DI s16x4 lds_tr(unsigned a) { return __builtin_amdgcn_ds_read_tr16_b64_v4i16((LAS s16x4*)(size_t)a); }
DI u32x2 lds_b64(unsigned a) { return *(LAS u32x2*)(size_t)a; }
DI void lds_w64(unsigned a, u32x2 v) { *(LAS u32x2*)(size_t)a = v; }
DI bf16x8 cat(s16x4 lo, s16x4 hi) { return __builtin_shufflevector(lo, hi, 0, 1, 2, 3, 4, 5, 6, 7); }
DI bf16x8 mk8(u32x2 a, u32x2 b) {
  u32x4 t = {a[0], a[1], b[0], b[1]};
  return __builtin_bit_cast(bf16x8, t);
}
typedef __amdgpu_buffer_rsrc_t rsrc_t;
DI void st128_sc1(rsrc_t r, unsigned off, u32x4 v) { __builtin_amdgcn_raw_buffer_store_b128(v, r, off, 0, 16); }
DI void st64_sc1(rsrc_t r, unsigned off, u32x2 v) { __builtin_amdgcn_raw_buffer_store_b64(v, r, off, 0, 16); }

DI u32x4 mish8(const f32x16& v, int o) {
  f32x2 h[4], z[4], e[4], p[4], d[4], r[4];
  const f32x2 c1 = {1.44269504088896f / 160.f, 1.44269504088896f / 160.f}, c0 = {129.f / 160.f, 129.f / 160.f};
#pragma unroll
  for (int k = 0; k < 4; ++k) { h[k] = f32x2{v[o + 2 * k], v[o + 2 * k + 1]}; asm("v_pk_fma_f32 %0, %1, %2, %3 clamp" : "=v"(z[k]) : "v"(h[k]), "v"(c1), "v"(c0)); }
#pragma unroll
  for (int k = 0; k < 4; ++k) z[k] = z[k] * 160.f - 129.f;
  SCHED();
#pragma unroll
  for (int k = 0; k < 4; ++k) e[k] = f32x2{__builtin_amdgcn_exp2f(z[k][0]), __builtin_amdgcn_exp2f(z[k][1])};
  SCHED();
#pragma unroll
  for (int k = 0; k < 4; ++k) p[k] = e[k] + 2.f;
#pragma unroll
  for (int k = 0; k < 4; ++k) d[k] = e[k] * p[k] + 2.f;
  SCHED();
#pragma unroll
  for (int k = 0; k < 4; ++k) r[k] = f32x2{__builtin_amdgcn_rcpf(d[k][0]), __builtin_amdgcn_rcpf(d[k][1])};
  SCHED();
#pragma unroll
  for (int k = 0; k < 4; ++k) h[k] = h[k] * e[k];
#pragma unroll
  for (int k = 0; k < 4; ++k) h[k] = h[k] * p[k];
#pragma unroll
  for (int k = 0; k < 4; ++k) h[k] = h[k] * r[k];
  u32x4 o4;
#pragma unroll
  for (int k = 0; k < 4; ++k) o4[k] = pack_bf16(h[k][0], h[k][1]);
  SCHED();
  return o4;
}
DI float xg_max(float v) {
  unsigned u = __float_as_uint(v);
  auto r = __builtin_amdgcn_permlane32_swap(u, u, false, false);
  v = fmaxf(__uint_as_float(r[0]), __uint_as_float(r[1]));
  u = __float_as_uint(v);
  auto r2 = __builtin_amdgcn_permlane16_swap(u, u, false, false);
  return fmaxf(__uint_as_float(r2[0]), __uint_as_float(r2[1]));
}
DI float xg_sum(float v) {
  unsigned u = __float_as_uint(v);
  auto r = __builtin_amdgcn_permlane32_swap(u, u, false, false);
  v = __uint_as_float(r[0]) + __uint_as_float(r[1]);
  u = __float_as_uint(v);
  auto r2 = __builtin_amdgcn_permlane16_swap(u, u, false, false);
  return __uint_as_float(r2[0]) + __uint_as_float(r2[1]);
}

template <int NM, int D>
constexpr int p2_allowed(int t) {
  int n = 0;
  for (int st = t + 2; st <= t + D - 2 && st < NST; ++st) n += st < D - 1 ? NM : NM + 2;
  return n;
}
template <int I, int N, class F>
DI void static_for(F&& f) {
  if constexpr (I < N) { f(std::integral_constant<int, I>{}); static_for<I + 1, N>(f); }
}
struct WLoad { unsigned voffn, voffe; const char* src; int st; };
template <int NM>
DI WLoad p2_w_prologue(unsigned smb, int l, int lane, int ct, int s0, const u16* __restrict__ w2) {
  const int r8 = lane >> 3;
  const unsigned colb = ct * 128 + (((lane & 7) ^ (((r8 >> 1) & 1) << 2)) * 16);
  WLoad s;
  s.voffn = r8 * 1280 + colb;
  s.voffe = (20 * (r8 >> 2) + (r8 & 3)) * 1280 + colb;
  s.src = (const char*)w2 + (16 * l) * 1280;
  const unsigned d = smb + Ring<NM>::W_RING + l * 2048;
  s.st = s0;
#pragma unroll 1
  for (int t = 0; t < Ring<NM>::D - 1; ++t) {
    const char* p = s.src + s.st * (64 * 1280);
    glds16(p, s.voffn, d + t * W_SLOT);
    glds16(p + 8 * 1280, s.voffn, d + t * W_SLOT + 1024);
    s.st = s.st == 26 ? 0 : s.st + 1;
  }
  return s;
}
DI void spin_flags(GAS u64* fp, u64 tag) {
#pragma unroll 1
  for (int spin = 0; spin < SPIN_CAP; ++spin) {
    if (__all(__hip_atomic_load(fp, RLX_AGENT) == tag)) break;
    __builtin_amdgcn_s_sleep(2);
  }
  asm volatile("" ::: "memory");
}
template <int NM>
DI void p2_loader(unsigned smb, int l, int lane, int m0, int sa, WLoad& ws, const char* __restrict__ mid, const u16* __restrict__ wo,
                  GAS u64* fpm, GAS u64* fpq, u64 tag, GAS u64* myflag) {
  constexpr int D = Ring<NM>::D, A_SLOT = Ring<NM>::A_SLOT;
  const unsigned lane16 = lane * 16;
  const char* src = mid + (size_t)((40 + l) * 64 + m0) * 1024;
  const unsigned da = smb + l * (NM * 1024), dw = smb + Ring<NM>::W_RING + l * 2048;
  auto issue_a = [&](unsigned slot) {
    const char* p = src + (size_t)sa * (4 * 65536);
#pragma unroll
    for (int m = 0; m < NM; ++m) glds16_sc1(p + m * 1024, lane16, da + slot * A_SLOT + m * 1024);
    sa = sa == 26 ? 0 : sa + 1;
  };
  spin_flags(fpm, tag);
#pragma unroll 1
  for (int t = 0; t < D - 1; ++t) issue_a(t);
  WAIT_VMI(NM * (D - 2));
  BARRIER_RAW();
  if (NM == 2 && l == 2 && lane == 0) {
    asm volatile("global_store_dwordx2 %0, %1, off sc1" : : "v"(myflag), "v"(tag) : "memory");
  }
  unsigned ps = D - 1;
  u64 fq = 0;
  auto issue_act = [&]() {
    const char* p = ws.src + sa * (64 * 1280);
    glds16(p, ws.voffn, dw + ps * W_SLOT);
    glds16(p + 8 * 1280, ws.voffn, dw + ps * W_SLOT + 1024);
    issue_a(ps);
    ps = (ps == D - 1) ? 0 : ps + 1;
  };
  static_for<0, D - 3>([&](auto tc) { WAIT_VMI((p2_allowed<NM, D>(tc.value))); BARRIER_RAW(); issue_act(); });
#pragma unroll 1
  for (int t = D - 3; t + D - 1 <= 24; ++t) { WAIT_VMI((NM + 2) * (D - 3)); BARRIER_RAW(); issue_act(); }
  asm volatile("global_load_dwordx2 %0, %1, off sc1" : "=v"(fq) : "v"(fpq) : "memory");
  WAIT_VMI((NM + 2) * (D - 3)); BARRIER_RAW(); issue_act();
  WAIT_VMI((NM + 2) * (D - 3)); BARRIER_RAW(); issue_act();
  asm volatile("s_waitcnt vmcnt(%1)" : "+v"(fq) : "i"(2 * (NM + 2)) : "memory");
  if (!__all(fq == tag)) spin_flags(fpq, tag);
  src = mid + (size_t)(l * 64 + m0) * 1024; sa = 0;
  int u = l;
  const char* won = (const char*)wo;
#pragma unroll 1
  for (int j = 0; j < 10; ++j) {
    WAIT_VMI((NM + 2) * (D - 3)); BARRIER_RAW();
    const int r0 = u < 4 ? 40 * (u >> 1) + 8 * (u & 1) : 16, dr = u < 4 ? 20 : 40;
    const unsigned vo = u < 4 ? ws.voffn : ws.voffe;
    glds16(won + r0 * 1280, vo, dw + ps * W_SLOT);
    glds16(won + (r0 + dr) * 1280, vo, dw + ps * W_SLOT + 1024);
    won += u >= 1 ? 80 * 1280 : 0;
    u = u >= 1 ? u - 1 : 4;
    issue_a(ps);
    ps = (ps == D - 1) ? 0 : ps + 1;
  }
  static_for<NST - D + 1, NST - 1>([&](auto tc) { WAIT_VMI((p2_allowed<NM, D>(tc.value))); BARRIER_RAW(); });
}

DI void qkv_slot(int sg, int odd, int& P, int& beta) {
  if (sg < 12) { P = sg >> 2; beta = (sg & 3) + odd; }
  else { P = sg < 15 ? sg - 12 : 2; beta = odd ? 0 : 4; }
}
template <int QKV>
DI void p1_loader(unsigned smb, int L, int lane, const char* __restrict__ x, const char* __restrict__ wmat, int row0, int cg, int ps, int np, int kk) {
  constexpr unsigned RB = QKV ? 3840 : 3456;
  constexpr int NL = QKV ? 13 : 12;
  unsigned vx = (row0 + 8 * L + (lane >> 3)) * 1280 + (((lane & 7) ^ (4 * (L & 1) + (lane >> 4))) * 16);
  const int hr = 4 * L + ((lane >> 3) & 3);
  int tokh = row0 - 16 + hr; tokh = tokh < 0 ? 0 : tokh;
  unsigned vh = tokh * 1280 + (((lane & 7) ^ ((hr >> 1) & 7)) * 16);
  const int hi = lane >> 5;
  unsigned vw[2];
#pragma unroll
  for (int e = 0; e < 2; ++e) {
    const int c = (lane & 31) ^ ((((2 * L + hi) & 3) << 2) | (e << 1));
    unsigned col;
    if (QKV) { int P, beta; qkv_slot(c >> 1, cg & 1, P, beta); col = (P == 2 ? 0 : (P + 1) * 1280) + cg * 160 + beta * 32 + (c & 1) * 16; }
    else col = ps * 64 + (c < 4 * np ? c : 4 * np - 1) * 16;
    vw[e] = (2 * L + hi) * RB + col;
  }
  auto issue = [&](unsigned buf) {
    const unsigned d = smb + buf + L * 1024;
    const char* xs = x + kk * 128;
    const char* wsrc = wmat + kk * (64 * RB);
#pragma unroll
    for (int j = 0; j < 8; ++j) {
      glds16(wsrc + j * (8 * RB), vw[j & 1], d + W_OFF + j * 4096);
      if (j < 4) glds16(xs + j * (32 * 1280), vx, d + 2048 + j * 4096);
      if (QKV && j == 4) glds16_lo(xs, vh, smb + buf + L * 512);
    }
    kk = kk == 9 ? 0 : kk + 1;
  };
  issue(0); issue(K1_STAGE);
  unsigned nb = 2 * K1_STAGE;
#pragma unroll 1
  for (int t = -1; t < 8; ++t) {
    WAIT_VMI(NL);
    BARRIER_RAW();
    if (t <= 6) issue(nb);
    nb = (nb == 2 * K1_STAGE) ? 0 : nb + K1_STAGE;
  }
  WAIT_VM(0);
  BARRIER_RAW();
}

struct Frag { bf16x8 x[2]; s16x4 lo[4], hi[4]; };
struct HFrag { bf16x8 x; s16x4 lo[3], hi[3]; };

template <int NMM>
DI void p1_read(Frag& F, int idx, unsigned ax0, unsigned ax1, const unsigned (&aw)[4], int s) {
  if (idx == 0) F.x[0] = lds_b128(ax0);
  else if (idx == 3) F.x[1] = lds_b128(ax1);
  else if (idx < (NMM > 6 ? 10 : 8)) {
    const int k = idx < 3 ? idx - 1 : idx - 2;
    if ((k & 1) == 0) F.lo[k >> 1] = lds_tr(aw[k >> 1] + W_OFF + s * 8192);
    else F.hi[k >> 1] = lds_tr(aw[k >> 1] + W_OFF + s * 8192 + 2048);
  }
}

template <int NMM, int NH>
DI void p1_compute(f32x16 (&acc)[2][4], f32x4 (&ah)[3], unsigned smb, unsigned lx0, unsigned lx1, const unsigned (&lw)[4],
                   unsigned lxh, const unsigned (&lh)[3] TSARG) {
  Frag F[2];
  HFrag H;
  unsigned aw[4];
#pragma unroll
  for (int n = 0; n < 4; ++n) aw[n] = smb + lw[n];
  BARRIER_RAW();
  ST(1);
#pragma unroll
  for (int i = 0; i < 10; ++i) p1_read<NMM>(F[0], i, smb + lx0, smb + lx1, aw, 0);
  unsigned cur = smb;
#pragma unroll 1
  for (int t = 0; t < 10; ++t) {
    const unsigned nxt = (cur == smb + 2 * K1_STAGE) ? smb : cur + K1_STAGE;
#pragma unroll
    for (int s = 0; s < 4; ++s) {
      const Frag& C = F[s & 1];
      Frag& N = F[(s + 1) & 1];
      const unsigned nb = s == 3 ? nxt : cur;
      const int ns = (s + 1) & 3;
      const unsigned ax0 = nb + (lx0 ^ (32 * ns)), ax1 = nb + (lx1 ^ (32 * ns));
#pragma unroll
      for (int mm = 0; mm < NMM; ++mm) {
        const int n = mm >> 1, m = mm & 1;
        acc[m][n] = MFMA32(cat(C.lo[n], C.hi[n]), C.x[m], acc[m][n]);
        if (s == 3 && mm == 0) {
          SCHED();
          BARRIER();
#pragma unroll
          for (int q = 0; q < 4; ++q) aw[q] = nxt + lw[q];
        }
        p1_read<NMM>(N, 2 * mm, ax0, ax1, aw, ns); p1_read<NMM>(N, 2 * mm + 1, ax0, ax1, aw, ns);
        if (NH) {
          if (mm == 4 && (s == 1 || s == 2)) {
#pragma unroll
            for (int j = 0; j < NH; ++j) ah[j] = MFMA16(cat(H.lo[j], H.hi[j]), H.x, ah[j]);
          }
          if (mm >= 5 && s < 2) {
            if (mm == 5) H.x = lds_b128(cur + (lxh ^ (64 * s)));
            if (mm - 5 < NH) { H.lo[mm - 5] = lds_tr(cur + lh[mm - 5] + W_OFF + s * 16384); H.hi[mm - 5] = lds_tr(cur + lh[mm - 5] + W_OFF + s * 16384 + 2048); }
          }
        }
        SCHED();
      }
    }
    cur = nxt;
  }
  ST(2);
}

DI unsigned p1_lx(int m32, int lane) {
  const int r = lane & 31;
  return (16 + 32 * m32 + r) * 128 + ((((lane >> 5)) ^ ((r >> 1) & 7)) * 16);
}
DI unsigned p1_lw(int T, int lane) {
  const int h = lane >> 5, q = (lane >> 2) & 3, p = lane & 3, cb = (lane >> 4) & 1;
  return (8 * h + q) * 512 + ((64 * T + 32 * cb + 8 * p) ^ ((q << 6) | (h << 5)));
}

template <int NMM>
DI void p1_mlp(unsigned smb, int w, int lane, int f0, int rt, const u16* __restrict__ b1, rsrc_t ws TSARG) {
  const int h = lane >> 5;
  const int nh = w >> 1;
  const int ma = 2 * (w & 1) + nh, mb = ma ^ 1;
  const int T[4] = {3 * nh, 3 * nh + 1, 3 * nh + 2, 6};
  f32x16 acc[2][4]; f32x4 ah[3];
  unsigned lw[4];
  u32x2 bv[4][4];
#pragma unroll
  for (int n = 0; n < 4; ++n)
#pragma unroll
    for (int a = 0; a < 4; ++a)
      if (n < 3 || NMM > 6) bv[n][a] = *(const u32x2*)(b1 + f0 + 32 * T[n] + 8 * a + 4 * h);
  SCHED();
#pragma unroll
  for (int n = 0; n < 4; ++n) {
    lw[n] = p1_lw(T[n], lane);
    if (n < 3 || NMM > 6) {
#pragma unroll
      for (int a = 0; a < 4; ++a) {
        acc[0][n][4 * a] = bf_lo(bv[n][a][0]); acc[0][n][4 * a + 1] = bf_hi(bv[n][a][0]);
        acc[0][n][4 * a + 2] = bf_lo(bv[n][a][1]); acc[0][n][4 * a + 3] = bf_hi(bv[n][a][1]);
      }
      acc[1][n] = acc[0][n];
      asm volatile("" : "+v"(acc[0][n]), "+v"(acc[1][n]));
    }
  }
  const unsigned lh[3] = {0u, 0u, 0u};
  p1_compute<NMM, 0>(acc, ah, smb, p1_lx(ma, lane), p1_lx(mb, lane), lw, 0u, lh TSPASS);
  asm volatile("s_nop 15\n\ts_nop 15" : "+v"(acc[0][0]), "+v"(acc[1][0]), "+v"(acc[0][1]), "+v"(acc[1][1]), "+v"(acc[0][2]), "+v"(acc[1][2]));
  if (NMM > 6) asm volatile("" : "+v"(acc[0][3]));
#pragma unroll
  for (int mm = 0; mm < NMM; ++mm) {
    const int n = mm >> 1, m = mm & 1;
    const unsigned dst = ((40 + (f0 >> 4) + 2 * T[n]) * 64 + rt * 4 + (m ? mb : ma)) * 1024 + lane * 16;
#pragma unroll
    for (int u = 0; u < 2; ++u) {
      const u32x4 o = mish8(acc[m][n], 8 * u);
      st128_sc1(ws, dst + u * 65536, o);
    }
  }
}

constexpr int XMT = 7680, XDUMP = 9 * XMT;
DI unsigned xslot_off(int sg, int odd) {
  int part, beta; qkv_slot(sg, odd, part, beta);
  return part * 2560 + beta * 512;
}

template <int NH>
DI void p1_qkv(unsigned smb, int w, int lane, int odd TSARG) {
  const int h = lane >> 5, g = lane >> 4, i = lane & 15;
  const int ma = 2 * (w & 1), T0 = 4 * (w >> 1);
  f32x16 acc[2][4] = {}; f32x4 ah[3] = {};
  const unsigned lxh = i * 128 + ((g ^ (i >> 1)) * 16);
  const int q = (lane >> 2) & 3, p = lane & 3;
  const unsigned hrow = (8 * g + q) * 512, hsw = (q << 6) | ((g & 1) << 5);
  const int hs[3] = {2 * w, 2 * w + 1, 12 + (w & 1)};
  unsigned lh[3], lw[4];
#pragma unroll
  for (int j = 0; j < 3; ++j) lh[j] = hrow + ((32 * hs[j] + 8 * p) ^ hsw);
#pragma unroll
  for (int n = 0; n < 4; ++n) { lw[n] = p1_lw(T0 + n, lane); asm volatile("" : "+v"(acc[0][n]), "+v"(acc[1][n])); }
  p1_compute<8, NH>(acc, ah, smb, p1_lx(ma, lane), p1_lx(ma + 1, lane), lw, lxh, lh TSPASS);
  const unsigned lb = smb + (1 + 2 * ma + (g & 1)) * XMT + h * 128 + i * 8;
#pragma unroll
  for (int n = 0; n < 4; ++n)
#pragma unroll
    for (int a2 = 0; a2 < 2; ++a2) {
      const int sg = 2 * (T0 + n) + a2;
      if (n == 3 && a2 == 1 && sg == 15) continue;
      const unsigned xo = lb + xslot_off(sg, odd);
#pragma unroll
      for (int a1 = 0; a1 < 2; ++a1)
#pragma unroll
        for (int m = 0; m < 2; ++m) {
          const int a = 2 * a2 + a1;
          lds_w64(xo + a1 * 256 + m * 2 * XMT, u32x2{pack_bf16(acc[m][n][4 * a], acc[m][n][4 * a + 1]), pack_bf16(acc[m][n][4 * a + 2], acc[m][n][4 * a + 3])});
        }
    }
#pragma unroll
  for (int j = 0; j < NH; ++j)
    lds_w64(smb + xslot_off(hs[j], odd) + lane * 8, u32x2{pack_bf16(ah[j][0], ah[j][1]), pack_bf16(ah[j][2], ah[j][3])});
}

DI void p1_attn(unsigned smb, int w, int lane, int rt, int hg, rsrc_t ws) {
  const int g = lane >> 4, i = lane & 15;
  const unsigned base = smb + w * XMT;
  const unsigned pa = base + lane * 8, pe = base + (5 * g + 4) * 128 + i * 8;
  const unsigned va = base + 2560 + (lane & 3) * 128 + (lane >> 2) * 8, ve = base + 2560 + (5 * (lane & 3) + 4) * 128 + (lane >> 2) * 8;
  u32x2 q[5], kp[5], kc[5], vp[5], vc[5];
#pragma unroll
  for (int t = 0; t < 5; ++t) {
    const unsigned a = t < 4 ? pa + t * 640 : pe;
    q[t] = lds_b64(a + XMT + 5120);
    kp[t] = lds_b64(a); kc[t] = lds_b64(a + XMT);
  }
#pragma unroll
  for (int t = 0; t < 5; ++t) {
    const unsigned a = t < 4 ? va + t * 640 : ve;
    vp[t] = __builtin_bit_cast(u32x2, lds_tr(a)); vc[t] = __builtin_bit_cast(u32x2, lds_tr(a + XMT));
  }
  const int par = i & 1;
  const bool c0 = (4 * g + par) <= i, c1 = (4 * g + par + 2) <= i;
  const bool first = (rt == 0) && (w == 0);
  const unsigned hm = par ? 0xffff0000u : 0x0000ffffu;
  const unsigned m_p0 = c0 ? 0u : hm, m_c0 = c0 ? hm : 0u, m_p1 = c1 ? 0u : hm, m_c1 = c1 ? hm : 0u;
  const float cs = 0.22360679774997896f * 1.44269504088896f;
  const f32x4 z4 = {0.f, 0.f, 0.f, 0.f};
  f32x4 oe = z4;
  float inv_e = 0.f;
  u32x2 op[4];
#pragma unroll
  for (int j = 0; j < 4; ++j) {
    u32x2 qe = q[4]; if (g != j) qe = u32x2{0u, 0u};
    bf16x8 qop = mk8(q[j], qe);
    f32x4 sp = MFMA16(mk8(kp[j], kp[4]), qop, z4);
    f32x4 sc = MFMA16(mk8(kc[j], kc[4]), qop, z4);
    float a0 = par ? sc[1] : sc[0], a1 = par ? sc[3] : sc[2];
    float b0 = par ? sp[1] : sp[0], b1 = par ? sp[3] : sp[2];
    if (first) { b0 = -INFINITY; b1 = -INFINITY; }
    float s0 = c0 ? a0 : b0, s1 = c1 ? a1 : b1;
    float mx = xg_max(fmaxf(s0, s1));
    float nm = -mx * cs;
    float p0 = __builtin_amdgcn_exp2f(fmaf(s0, cs, nm)), p1 = __builtin_amdgcn_exp2f(fmaf(s1, cs, nm));
    unsigned w0 = pack_bf16(p0, p0), w1 = pack_bf16(p1, p1);
    float sum = xg_sum(bf_lo(w0) + bf_lo(w1));
    float inv = __builtin_amdgcn_rcpf(sum);
    u32x4 pw = {w0 & m_p0, w1 & m_p1, w0 & m_c0, w1 & m_c1};
    bf16x8 pop = __builtin_bit_cast(bf16x8, pw);
    f32x4 o = MFMA16(mk8(vp[j], vc[j]), pop, z4);
    u32x2 ep = vp[4], ec = vc[4];
    if ((i >> 2) != j) { ep = u32x2{0u, 0u}; ec = u32x2{0u, 0u}; }
    oe = MFMA16(mk8(ep, ec), pop, oe);
    if (g == j) inv_e = inv;
    op[j] = u32x2{pack_bf16(o[0] * inv, o[1] * inv), pack_bf16(o[2] * inv, o[3] * inv)};
  }
  const int m32 = rt * 4 + (w >> 1);
  const unsigned lp = (32 * (g & 1) + 16 * (w & 1) + i) * 16;
  const unsigned d0 = ((5 * hg + (g >> 1)) * 64 + m32) * 1024 + lp;
  st128_sc1(ws, d0, u32x4{op[0][0], op[0][1], op[1][0], op[1][1]});
  st128_sc1(ws, d0 + 2 * 65536, u32x4{op[2][0], op[2][1], op[3][0], op[3][1]});
  st64_sc1(ws, ((5 * hg + 4) * 64 + m32) * 1024 + lp + (g >> 1) * 8,
           u32x2{pack_bf16(oe[0] * inv_e, oe[1] * inv_e), pack_bf16(oe[2] * inv_e, oe[3] * inv_e)});
}

template <int NM> struct Frag2 { bf16x8 a[2][NM]; s16x4 lo[2], hi[2]; };
template <int NM>
DI void p2_stage(const Frag2<NM>& C, Frag2<NM>& N, unsigned pa, unsigned pw, f32x16 (&acc)[NM]) {
#pragma unroll
  for (int k = 0; k < 2; ++k) {
#pragma unroll
    for (int m = 0; m < NM; ++m) {
      acc[m] = MFMA32(cat(C.lo[k], C.hi[k]), C.a[k][m], acc[m]);
      if (m == 0) { N.lo[k] = lds_tr(pw + k * 2048); N.hi[k] = lds_tr(pw + k * 2048 + 1024); }
      N.a[k][m] = lds_b128(pa + (k * NM + m) * 1024);
      SCHED();
    }
  }
}
constexpr int RED_OFF = 16384;
template <int NM>
DI void p2_compute(unsigned smb, int w, int lane, int m0, GAS float* gate TSARG) {
  const int kh = w >> 1, nh = w & 1, h = lane >> 5;
  constexpr int D = Ring<NM>::D, A_SLOT = Ring<NM>::A_SLOT;
  const unsigned la = smb + kh * (2 * NM * 1024) + lane * 16;
  const int q = (lane >> 2) & 3;
  const unsigned lw = smb + Ring<NM>::W_RING + kh * 4096 + (4 * h + q) * 128 + ((64 * nh + 32 * ((lane >> 4) & 1) + 8 * (lane & 3)) ^ ((q >> 1) << 6));
  f32x16 accA[NM] = {}, accB[NM] = {};
  Frag2<NM> F0, F1;
  WAIT_VM(0);
  BARRIER_RAW();
  ST(7);
  float gv[NM];
#pragma unroll
  for (int m = 0; m < NM; ++m) gv[m] = __hip_atomic_load(gate + (m0 + m) * 32 + (lane & 31), RLX_AGENT);
#pragma unroll
  for (int k = 0; k < 2; ++k) {
    F0.lo[k] = lds_tr(lw + k * 2048); F0.hi[k] = lds_tr(lw + k * 2048 + 1024);
#pragma unroll
    for (int m = 0; m < NM; ++m) F0.a[k][m] = lds_b128(la + (k * NM + m) * 1024);
  }
  unsigned ca = A_SLOT, cw = W_SLOT;
  auto adv = [&]() { ca = (ca == (D - 1) * A_SLOT) ? 0 : ca + A_SLOT; cw = (cw == (D - 1) * W_SLOT) ? 0 : cw + W_SLOT; };
#pragma unroll 1
  for (int t = 0; t < 26; t += 2) {
    BARRIER_RAW(); p2_stage<NM>(F0, F1, la + ca, lw + cw, accB); adv();
    BARRIER_RAW(); p2_stage<NM>(F1, F0, la + ca, lw + cw, accB); adv();
  }
  BARRIER_RAW(); p2_stage<NM>(F0, F1, la + ca, lw + cw, accB); adv();
  ST(8);
  BARRIER_RAW(); p2_stage<NM>(F1, F0, la + ca, lw + cw, accA); adv();
#pragma unroll 1
  for (int t = 28; t < NST - 1; t += 2) {
    BARRIER_RAW(); p2_stage<NM>(F0, F1, la + ca, lw + cw, accA); adv();
    BARRIER_RAW(); p2_stage<NM>(F1, F0, la + ca, lw + cw, accA); adv();
  }
#pragma unroll
  for (int k = 0; k < 2; ++k)
#pragma unroll
    for (int m = 0; m < NM; ++m) accA[m] = MFMA32(cat(F0.lo[k], F0.hi[k]), F0.a[k][m], accA[m]);
  ST(9);
  BARRIER();
  f32x4 keep[NM][2];
#pragma unroll
  for (int m = 0; m < NM; ++m) {
    const float hv = 1.f - gv[m];
#pragma unroll
    for (int a = 0; a < 2; ++a) {
      f32x4 lo, hi;
#pragma unroll
      for (int e = 0; e < 4; ++e) {
        lo[e] = gv[m] * accA[m][4 * a + e] + hv * accB[m][4 * a + e];
        hi[e] = gv[m] * accA[m][8 + 4 * a + e] + hv * accB[m][8 + 4 * a + e];
      }
      keep[m][a] = kh ? hi : lo;
      *(LAS f32x4*)(size_t)(smb + RED_OFF + ((w * NM + m) * 2 + a) * 1024 + lane * 16) = kh ? lo : hi;
    }
  }
  BARRIER();
#pragma unroll
  for (int m = 0; m < NM; ++m)
#pragma unroll
    for (int a = 0; a < 2; ++a) {
      const f32x4 o = keep[m][a] + *(LAS f32x4*)(size_t)(smb + RED_OFF + (((w ^ 2) * NM + m) * 2 + a) * 1024 + lane * 16);
      lds_w64(smb + (m * 32 + (lane & 31)) * 144 + nh * 64 + (2 * kh + a) * 16 + h * 8, u32x2{pack_bf16(o[0], o[1]), pack_bf16(o[2], o[3])});
    }
}

__global__ __launch_bounds__(NTHREADS, 2) void fused_kernel(const u16* __restrict__ x, const u16* __restrict__ wqkv, const u16* __restrict__ wo,
                                                            const u16* __restrict__ w1, const u16* __restrict__ b1, const u16* __restrict__ w2,
                                                            const u16* __restrict__ gw, char* __restrict__ wsp, u16* __restrict__ out) {
  extern __shared__ __attribute__((aligned(16))) char smem[];
  const unsigned smb = (unsigned)(size_t)(LAS char*)smem;
  TSDECL; ST(0);
  const int lane = threadIdx.x & 63;
  const int w = __builtin_amdgcn_readfirstlane(threadIdx.x >> 6);
  const int b = blockIdx.x, xcd = b & 7, idx = b >> 3;
  const int is_mlp = idx & 1;
  const int rt = (xcd >> 1) * 4 + ((idx >> 1) & 3);
  const int cg = (xcd & 1) * 4 + (idx >> 3);
  const int row0 = rt * 128;
  const int ps = cg < 7 ? 7 * cg : 48, np = cg < 6 ? 7 : 6;
  int ct, m0;
  {
    const int j = idx >> 1;
    if (is_mlp) { if (j < 12) { ct = j >> 2; m0 = 3 * (j & 3); } else { ct = 3 + ((j - 12) >> 1); m0 = 3 * (j & 1); } }
    else { if (j < 6) { ct = j >> 1; m0 = 12 + 2 * (j & 1); } else { const int jj = j - 6; ct = 3 + (jj >= 5); m0 = 6 + 2 * (jj >= 5 ? jj - 5 : jj); } }
    ct += (xcd & 1) * 5; m0 += (xcd >> 1) * 16;
  }
  const int nm = is_mlp ? 3 : 2;
  const rsrc_t ws = __builtin_amdgcn_make_buffer_rsrc(wsp, 0, WS_BYTES, 0x00020000);
  GAS u64* flags = (GAS u64*)(wsp + FLAG_OFF);
  GAS u64* epoch = (GAS u64*)(wsp + EPOCH_OFF);
  GAS float* gate = (GAS float*)(wsp + GATE_OFF);

#define ROT1 1
#define ROT2 1
  int k0 = ((5 * (rt & 3)) >> 1) + ((5 * (is_mlp * 4 + (cg & 3))) >> 2);
  k0 = ROT1 ? (k0 >= 10 ? k0 - 10 : k0) : 0;
  int s0 = ROT2 ? ((m0 & 15) * 27) >> 4 : 0;
  s0 += ROT2 ? (ct % 5) * 5 : 0; s0 = s0 >= 27 ? s0 - 27 : s0;
  WLoad wl = {0u, 0u, nullptr, 0};
  u64 tag = 0;
  if (w >= 4) {
    if (is_mlp) p1_loader<0>(smb, w - 4, lane, (const char*)x, (const char*)w1, row0, cg, ps, np, k0);
    else p1_loader<1>(smb, w - 4, lane, (const char*)x, (const char*)wqkv, row0, cg, ps, np, k0);
    wl = is_mlp ? p2_w_prologue<3>(smb, w - 4, lane, ct, s0, w2) : p2_w_prologue<2>(smb, w - 4, lane, ct, s0, w2);
    if (w < 6) {
      const int id = idx * 100 + (w - 4) * 50 + (lane < 50 ? lane : 49);
      const int row = id / 5;
      unsigned dm;
      asm volatile("global_load_dword %0, %1, %2" : "=v"(dm) : "v"((unsigned)(row * 1280 + ((xcd & 1) * 5 + (id - row * 5)) * 128)), "s"(wo) : "memory");
      BARRIER_RAW();
      asm volatile("s_waitcnt vmcnt(0)" : "+v"(dm) :: "memory");
    } else
    BARRIER_RAW();
    tag = __hip_atomic_load(epoch, RLX_AGENT) * 0x9E3779B97F4A7C15ull + 0x632BE59BD9B4E019ull;
    if (w >= 6) {
      if (is_mlp) {
        const int row = row0 + cg * 16 + (w - 6) * 8 + (lane >> 3);
        const char* xr = (const char*)x + row * 1280 + (lane & 7) * 16;
        const char* gr = (const char*)gw + (lane & 7) * 16;
        float s = 0.f;
#pragma unroll
        for (int t = 0; t < 10; ++t) {
          u32x4 a = *(const u32x4*)(xr + t * 128), c = *(const u32x4*)(gr + t * 128);
#pragma unroll
          for (int e = 0; e < 4; ++e) {
            const unsigned ua = a[e], uc = c[e];
            s = __builtin_amdgcn_fdot2_f32_bf16(__builtin_bit_cast(bf16x2_t, ua), __builtin_bit_cast(bf16x2_t, uc), s, false);
          }
        }
        s += __shfl_xor(s, 1); s += __shfl_xor(s, 2); s += __shfl_xor(s, 4);
        if ((lane & 7) == 0) __hip_atomic_store(gate + row, 1.f / (1.f + __expf(-s)), RLX_AGENT);
      }
    }
    ST(1); ST(2);
  } else if (is_mlp) {
    const int f0 = ps * 32;
    if (np == 7) p1_mlp<7>(smb, w, lane, f0, rt, b1, ws TSPASS);
    else p1_mlp<6>(smb, w, lane, f0, rt, b1, ws TSPASS);
  } else {
    if (w < 2) p1_qkv<3>(smb, w, lane, cg & 1 TSPASS);
    else p1_qkv<2>(smb, w, lane, cg & 1 TSPASS);
  }
  if (!is_mlp) {
    BARRIER();
    ST(3);
    p1_attn(smb, w, lane, rt, cg, ws);
  } else { ST(3); }
  ST(4);
  GAS u64* myflag = flags + rt * 16 + is_mlp * 8 + cg;
  if (is_mlp) WAIT_VM(0);
  BARRIER();
  ST(5);
  if (is_mlp && w == 6 && lane == 0) __hip_atomic_store(myflag, tag, RLX_AGENT);
  ST(6);
  if (w >= 4) {
    const int rta = m0 >> 2, rtb = (m0 + nm - 1) >> 2;
    GAS u64* fq = flags + ((lane & 8) ? rtb : rta) * 16 + (lane & 7);
    if (is_mlp) p2_loader<3>(smb, w - 4, lane, m0, s0, wl, wsp, wo, fq + 8, fq, tag, myflag);
    else p2_loader<2>(smb, w - 4, lane, m0, s0, wl, wsp, wo, fq + 8, fq, tag, myflag);
    ST(7); ST(8); ST(9);
    BARRIER_RAW();
    BARRIER_RAW();
  } else if (is_mlp) p2_compute<3>(smb, w, lane, m0, gate TSPASS);
  else p2_compute<2>(smb, w, lane, m0, gate TSPASS);
  BARRIER();
  for (int c = threadIdx.x; c < nm * 256; c += NTHREADS) {
    const int r = c >> 3, ch = c & 7;
    u32x4 v = *(LAS u32x4*)(size_t)(smb + r * 144 + ch * 16);
    *(u32x4*)((char*)out + (size_t)(m0 * 32 + r) * 1280 + ct * 128 + ch * 16) = v;
  }
  if (b == 254 && w == 7) {
    for (int spin = 0; spin < SPIN_CAP; ++spin) {
      bool ok = true;
#pragma unroll
      for (int k = 0; k < 4; ++k) ok &= __hip_atomic_load(flags + k * 64 + lane, RLX_AGENT) == tag;
      if (__all(ok)) break;
      __builtin_amdgcn_s_sleep(8);
    }
    if (lane == 0) __hip_atomic_store(epoch, tag, RLX_AGENT);
  }
}

extern "C" void kernel_launch(void* const* d_in, const int* in_sizes, int n_in, void* d_out, int out_size, void* d_ws,
                              size_t ws_size, hipStream_t stream) {
  (void)hipFuncSetAttribute((const void*)fused_kernel, hipFuncAttributeMaxDynamicSharedMemorySize, LDS_BYTES);
  fused_kernel<<<256, NTHREADS, LDS_BYTES, stream>>>((const u16*)d_in[0], (const u16*)d_in[1], (const u16*)d_in[2], (const u16*)d_in[4],
                                                     (const u16*)d_in[5], (const u16*)d_in[6], (const u16*)d_in[7], (char*)d_ws + ((ws_size - WS_BYTES) & ~(size_t)4095), (u16*)d_out);
}
```

```cpp
#include <hip/hip_runtime.h>
#include <stdint.h>
#include <type_traits>

typedef unsigned short u16;
typedef unsigned long long u64;
typedef short bf16x8 __attribute__((ext_vector_type(8)));
typedef short s16x4 __attribute__((ext_vector_type(4)));
typedef float f32x4 __attribute__((ext_vector_type(4)));
typedef float f32x16 __attribute__((ext_vector_type(16)));
typedef float f32x2 __attribute__((ext_vector_type(2)));
typedef unsigned u32x4 __attribute__((ext_vector_type(4)));
typedef unsigned u32x2 __attribute__((ext_vector_type(2)));
typedef unsigned u32x16 __attribute__((ext_vector_type(16)));
typedef __bf16 bf16x2_t __attribute__((ext_vector_type(2)));
#define LAS __attribute__((address_space(3)))
#define GAS __attribute__((address_space(1)))
#define DI __device__ __forceinline__

#define MFMA16(a, b, c) __builtin_amdgcn_mfma_f32_16x16x32_bf16((a), (b), (c), 0, 0, 0)
#define MFMA32(a, b, c) __builtin_amdgcn_mfma_f32_32x32x16_bf16((a), (b), (c), 0, 0, 0)
#define WAIT_VM(n) asm volatile("s_waitcnt vmcnt(" #n ")" ::: "memory")
#define WAIT_VMI(n) asm volatile("s_waitcnt vmcnt(%0)" ::"i"(n) : "memory")
#define BARRIER() do { asm volatile("s_waitcnt lgkmcnt(0)" ::: "memory"); __builtin_amdgcn_s_barrier(); asm volatile("" ::: "memory"); } while (0)
#define BARRIER_RAW() do { asm volatile("" ::: "memory"); __builtin_amdgcn_s_barrier(); asm volatile("" ::: "memory"); } while (0)
#define SCHED() __builtin_amdgcn_sched_barrier(0)
#define RLX_AGENT __ATOMIC_RELAXED, __HIP_MEMORY_SCOPE_AGENT

#define TSARG
#define TSPASS
#define TSDECL
#define ST(id)
#define TSOUT()

constexpr int NTHREADS = 512;
constexpr int NKAP = 148;
constexpr int W_OFF = 18432;
constexpr int K1_STAGE = 51200;
constexpr unsigned MID_BYTES = NKAP * 64 * 1024;
constexpr unsigned GATE_OFF = MID_BYTES, FLAG_OFF = GATE_OFF + 8192, EPOCH_OFF = FLAG_OFF + 2048, WS_BYTES = EPOCH_OFF + 128;
constexpr int LDS_BYTES = 163840;
constexpr int W_SLOT = 8192;
template <int NM> struct Ring {
  static constexpr int D = NM == 3 ? 8 : 10;
  static constexpr int A_SLOT = NM * 4096;
  static constexpr int W_RING = D * A_SLOT;
};
constexpr int NST = NKAP / 4;
constexpr int SPIN_CAP = 1 << 22;
constexpr int PFN = 1;
constexpr int CODE_CHUNKS = 11;

DI unsigned pack_bf16(float lo, float hi) {
  f32x2 f = {lo, hi};
  return __builtin_bit_cast(unsigned, __builtin_convertvector(f, bf16x2_t));
}
DI float bf_lo(unsigned u) { return __uint_as_float(u << 16); }
DI float bf_hi(unsigned u) { return __uint_as_float(u & 0xffff0000u); }
DI void glds16(const char* sbase, unsigned voff, unsigned dst) {
  asm volatile("s_mov_b32 m0, %2\n\ts_nop 0\n\tglobal_load_lds_dwordx4 %0, %1" : : "v"(voff), "s"(sbase), "s"(dst) : "memory");
}
DI void glds16_sc1(const char* sbase, unsigned voff, unsigned dst) {
  asm volatile("s_mov_b32 m0, %2\n\ts_nop 0\n\tglobal_load_lds_dwordx4 %0, %1 sc1" : : "v"(voff), "s"(sbase), "s"(dst) : "memory");
}
DI void glds16_lo(const char* sbase, unsigned voff, unsigned dst) {
  asm volatile("s_mov_b32 m0, %2\n\ts_mov_b32 exec_hi, 0\n\tglobal_load_lds_dwordx4 %0, %1\n\ts_mov_b32 exec_hi, -1" : : "v"(voff), "s"(sbase), "s"(dst) : "memory");
}
DI bf16x8 lds_b128(unsigned a) { return *(LAS bf16x8*)(size_t)a; }
DI s16x4 lds_tr(unsigned a) { return __builtin_amdgcn_ds_read_tr16_b64_v4i16((LAS s16x4*)(size_t)a); }
DI u32x2 lds_b64(unsigned a) { return *(LAS u32x2*)(size_t)a; }
DI void lds_w64(unsigned a, u32x2 v) { *(LAS u32x2*)(size_t)a = v; }
DI bf16x8 cat(s16x4 lo, s16x4 hi) { return __builtin_shufflevector(lo, hi, 0, 1, 2, 3, 4, 5, 6, 7); }
DI bf16x8 mk8(u32x2 a, u32x2 b) {
  u32x4 t = {a[0], a[1], b[0], b[1]};
  return __builtin_bit_cast(bf16x8, t);
}
typedef __amdgpu_buffer_rsrc_t rsrc_t;
#define STAUX 16
DI void st128_sc1(rsrc_t r, unsigned off, u32x4 v) { __builtin_amdgcn_raw_buffer_store_b128(v, r, off, 0, STAUX); }
DI void st64_sc1(rsrc_t r, unsigned off, u32x2 v) { __builtin_amdgcn_raw_buffer_store_b64(v, r, off, 0, 16); }

DI u32x4 mish8(const f32x16& v, int o) {
  f32x2 h[4], z[4], e[4], p[4], d[4], r[4];
  const f32x2 c1 = {1.44269504088896f / 160.f, 1.44269504088896f / 160.f}, c0 = {129.f / 160.f, 129.f / 160.f};
#pragma unroll
  for (int k = 0; k < 4; ++k) { h[k] = f32x2{v[o + 2 * k], v[o + 2 * k + 1]}; asm("v_pk_fma_f32 %0, %1, %2, %3 clamp" : "=v"(z[k]) : "v"(h[k]), "v"(c1), "v"(c0)); }
#pragma unroll
  for (int k = 0; k < 4; ++k) z[k] = z[k] * 160.f - 129.f;
  SCHED();
#pragma unroll
  for (int k = 0; k < 4; ++k) e[k] = f32x2{__builtin_amdgcn_exp2f(z[k][0]), __builtin_amdgcn_exp2f(z[k][1])};
  SCHED();
#pragma unroll
  for (int k = 0; k < 4; ++k) p[k] = e[k] + 2.f;
#pragma unroll
  for (int k = 0; k < 4; ++k) d[k] = e[k] * p[k] + 2.f;
  SCHED();
#pragma unroll
  for (int k = 0; k < 4; ++k) r[k] = f32x2{__builtin_amdgcn_rcpf(d[k][0]), __builtin_amdgcn_rcpf(d[k][1])};
  SCHED();
#pragma unroll
  for (int k = 0; k < 4; ++k) h[k] = h[k] * e[k];
#pragma unroll
  for (int k = 0; k < 4; ++k) h[k] = h[k] * p[k];
#pragma unroll
  for (int k = 0; k < 4; ++k) h[k] = h[k] * r[k];
  u32x4 o4;
#pragma unroll
  for (int k = 0; k < 4; ++k) o4[k] = pack_bf16(h[k][0], h[k][1]);
  SCHED();
  return o4;
}
DI float xg_max(float v) {
  unsigned u = __float_as_uint(v);
  auto r = __builtin_amdgcn_permlane32_swap(u, u, false, false);
  v = fmaxf(__uint_as_float(r[0]), __uint_as_float(r[1]));
  u = __float_as_uint(v);
  auto r2 = __builtin_amdgcn_permlane16_swap(u, u, false, false);
  return fmaxf(__uint_as_float(r2[0]), __uint_as_float(r2[1]));
}
DI float xg_sum(float v) {
  unsigned u = __float_as_uint(v);
  auto r = __builtin_amdgcn_permlane32_swap(u, u, false, false);
  v = __uint_as_float(r[0]) + __uint_as_float(r[1]);
  u = __float_as_uint(v);
  auto r2 = __builtin_amdgcn_permlane16_swap(u, u, false, false);
  return __uint_as_float(r2[0]) + __uint_as_float(r2[1]);
}

#define RAMP0 2
template <int D>
constexpr int p2_issued(int t) {
  int n = RAMP0;
  for (int i = 0; i <= t; ++i) { n += 2; if (n > i + D - 1) n = i + D - 1; }
  return n > NST ? NST : n;
}
template <int NM, int D>
constexpr int p2_allowed(int t) {
  int n = 0;
  for (int st = t + 2; st < p2_issued<D>(t); ++st) n += st < D - 1 ? NM : NM + 2;
  return n;
}
template <int I, int N, class F>
DI void static_for(F&& f) {
  if constexpr (I < N) { f(std::integral_constant<int, I>{}); static_for<I + 1, N>(f); }
}
struct WLoad { unsigned voffn, voffe; const char* src; int st; };
template <int NM>
DI WLoad p2_w_prologue(unsigned smb, int l, int lane, int ct, int s0, const u16* __restrict__ w2) {
  const int r8 = lane >> 3;
  const unsigned colb = ct * 128 + (((lane & 7) ^ (((r8 >> 1) & 1) << 2)) * 16);
  WLoad s;
  s.voffn = r8 * 1280 + colb;
  s.voffe = (20 * (r8 >> 2) + (r8 & 3)) * 1280 + colb;
  s.src = (const char*)w2 + (16 * l) * 1280;
  const unsigned d = smb + Ring<NM>::W_RING + l * 2048;
  s.st = s0;
#pragma unroll 1
  for (int t = 0; t < Ring<NM>::D - 1; ++t) {
    const char* p = s.src + s.st * (64 * 1280);
    glds16(p, s.voffn, d + t * W_SLOT);
    glds16(p + 8 * 1280, s.voffn, d + t * W_SLOT + 1024);
    s.st = s.st == 26 ? 0 : s.st + 1;
  }
  return s;
}
DI void spin_flags(GAS u64* fp, u64 tag) {
#pragma unroll 1
  for (int spin = 0; spin < SPIN_CAP; ++spin) {
    if (__all(__hip_atomic_load(fp, RLX_AGENT) == tag)) break;
    __builtin_amdgcn_s_sleep(2);
  }
  asm volatile("" ::: "memory");
}
template <int NM>
DI void p2_loader(unsigned smb, int l, int lane, int m0, int sa, WLoad& ws, const char* __restrict__ mid, const u16* __restrict__ wo,
                  GAS u64* fpm, GAS u64* fpq, u64 tag, GAS u64* myflag) {
  constexpr int D = Ring<NM>::D, A_SLOT = Ring<NM>::A_SLOT;
  const unsigned lane16 = lane * 16;
  const char* src = mid + (size_t)((40 + l) * 64 + m0) * 1024;
  const unsigned da = smb + l * (NM * 1024), dw = smb + Ring<NM>::W_RING + l * 2048;
  auto issue_a = [&](unsigned slot) {
    const char* p = src + (size_t)sa * (4 * 65536);
#pragma unroll
    for (int m = 0; m < NM; ++m) glds16_sc1(p + m * 1024, lane16, da + slot * A_SLOT + m * 1024);
    sa = sa == 26 ? 0 : sa + 1;
  };
  spin_flags(fpm, tag);
#pragma unroll
  for (int t = 0; t < RAMP0; ++t) issue_a(t);
  WAIT_VMI((p2_allowed<NM, D>(-1)));
  BARRIER_RAW();
  if (NM == 2 && l == 2 && lane == 0) {
    asm volatile("global_store_dwordx2 %0, %1, off sc1" : : "v"(myflag), "v"(tag) : "memory");
  }
  unsigned ps = D - 1;
  u64 fq = 0;
  auto issue_act = [&]() {
    const char* p = ws.src + sa * (64 * 1280);
    glds16(p, ws.voffn, dw + ps * W_SLOT);
    glds16(p + 8 * 1280, ws.voffn, dw + ps * W_SLOT + 1024);
    issue_a(ps);
    ps = (ps == D - 1) ? 0 : ps + 1;
  };
  auto issue_range = [&](auto c0, auto c1) {
    static_for<c0.value, c1.value>([&](auto sc) { if constexpr (sc.value < D - 1) issue_a(sc.value); else issue_act(); });
  };
  issue_range(std::integral_constant<int, p2_issued<D>(-1)>{}, std::integral_constant<int, p2_issued<D>(0)>{});
  static_for<0, D - 3>([&](auto tc) {
    WAIT_VMI((p2_allowed<NM, D>(tc.value))); BARRIER_RAW();
    issue_range(std::integral_constant<int, p2_issued<D>(tc.value)>{}, std::integral_constant<int, p2_issued<D>(tc.value + 1)>{});
  });
  static_assert(p2_issued<D>(D - 3) == 2 * D - 4, "ring must be full when the steady state starts");
#pragma unroll 1
  for (int t = D - 3; t + D - 1 <= 24; ++t) { WAIT_VMI((NM + 2) * (D - 3)); BARRIER_RAW(); issue_act(); }
  asm volatile("global_load_dwordx2 %0, %1, off sc1" : "=v"(fq) : "v"(fpq) : "memory");
  WAIT_VMI((NM + 2) * (D - 3)); BARRIER_RAW(); issue_act();
  WAIT_VMI((NM + 2) * (D - 3)); BARRIER_RAW(); issue_act();
  asm volatile("s_waitcnt vmcnt(%1)" : "+v"(fq) : "i"(2 * (NM + 2)) : "memory");
  if (!__all(fq == tag)) spin_flags(fpq, tag);
  src = mid + (size_t)(l * 64 + m0) * 1024; sa = 0;
  int u = l;
  const char* won = (const char*)wo;
#pragma unroll 1
  for (int j = 0; j < 10; ++j) {
    WAIT_VMI((NM + 2) * (D - 3)); BARRIER_RAW();
    const int r0 = u < 4 ? 40 * (u >> 1) + 8 * (u & 1) : 16, dr = u < 4 ? 20 : 40;
    const unsigned vo = u < 4 ? ws.voffn : ws.voffe;
    glds16(won + r0 * 1280, vo, dw + ps * W_SLOT);
    glds16(won + (r0 + dr) * 1280, vo, dw + ps * W_SLOT + 1024);
    won += u >= 1 ? 80 * 1280 : 0;
    u = u >= 1 ? u - 1 : 4;
    issue_a(ps);
    ps = (ps == D - 1) ? 0 : ps + 1;
  }
  static_for<NST - D + 1, NST - 1>([&](auto tc) { WAIT_VMI((p2_allowed<NM, D>(tc.value))); BARRIER_RAW(); });
}

DI void qkv_slot(int sg, int odd, int& P, int& beta) {
  if (sg < 12) { P = sg >> 2; beta = (sg & 3) + odd; }
  else { P = sg < 15 ? sg - 12 : 2; beta = odd ? 0 : 4; }
}
DI unsigned p1_vw(int v, int lane, int is_mlp, int cg, int ps, int np) {
  const int hi = lane >> 5;
  const int c = (lane & 31) ^ ((((2 * v + hi) & 3) << 2) | ((v >> 2) << 1));
  const int sg = c >> 1, odd = cg & 1;
  const int P = sg < 12 ? sg >> 2 : (sg < 15 ? sg - 12 : 2);
  const int beta = sg < 12 ? (sg & 3) + odd : (odd ? 0 : 4);
  const unsigned colq = (P == 2 ? 0 : (P + 1) * 1280) + cg * 160 + beta * 32 + (c & 1) * 16;
  const unsigned colm = ps * 64 + (c < 4 * np ? c : 4 * np - 1) * 16;
  return (2 * v + hi) * (is_mlp ? 3456u : 3840u) + (is_mlp ? colm : colq);
}
#define ROT1 2
#define ROT2 1
#define PRE1 5
DI int p1_next(int kk) { return kk == 9 ? (ROT1 == 2 ? 1 : 0) : kk + 1; }
template <int QKV>
DI void p1_loader(unsigned smb, int L, int lane, const char* __restrict__ x, const char* __restrict__ wmat, int row0, unsigned vw0, unsigned vw1, int kk TSARG) {
  constexpr unsigned RB = QKV ? 3840 : 3456;
  constexpr int NL = QKV ? 13 : 12;
  const unsigned vx = (row0 + 8 * L + (lane >> 3)) * 1280 + (((lane & 7) ^ (4 * (L & 1) + (lane >> 4))) * 16);
  const int hr = 4 * L + ((lane >> 3) & 3);
  int tokh = row0 - 16 + hr; tokh = tokh < 0 ? 0 : tokh;
  const unsigned vh = tokh * 1280 + (((lane & 7) ^ ((hr >> 1) & 7)) * 16);
  auto piece = [&](int p, unsigned buf) {
    const unsigned d = smb + buf + L * 1024;
    const char* xs = x + kk * 128;
    const char* wsrc = wmat + kk * (64 * RB);
    if (p < 8) { if (p & 1) glds16(xs + (p >> 1) * (32 * 1280), vx, d + 2048 + (p >> 1) * 4096);
                 else glds16(wsrc + (p >> 2) * (16 * RB), (p & 2) ? vw1 : vw0, d + W_OFF + (p >> 1) * 4096); }
    else if (p < 12) glds16(wsrc + ((p - 4) >> 1) * (16 * RB), (p & 1) ? vw1 : vw0, d + W_OFF + (p - 4) * 4096);
    else glds16_lo(xs, vh, smb + buf + L * 512);
  };
  auto issue = [&](unsigned buf) {
#pragma unroll
    for (int p = 0; p < NL; ++p) piece(p, buf);
    kk = p1_next(kk);
  };
#pragma unroll
  for (int p = 0; p < PRE1; ++p) piece(p, K1_STAGE);
  WAIT_VMI(PRE1 + PFN);
  BARRIER_RAW();
#pragma unroll
  for (int p = PRE1; p < NL; ++p) piece(p, K1_STAGE);
  kk = p1_next(kk);
  issue(2 * K1_STAGE);
  unsigned nb = 0;
#pragma unroll 1
  for (int t = 0; t < 7; ++t) {
    WAIT_VMI(NL);
    BARRIER_RAW();
    issue(nb);
    nb = (nb == 2 * K1_STAGE) ? 0 : nb + K1_STAGE;
  }
  WAIT_VMI(NL);
  BARRIER_RAW();
  WAIT_VM(0);
  BARRIER_RAW();
}

struct Frag { bf16x8 x[2]; s16x4 lo[4], hi[4]; };
struct HFrag { bf16x8 x; s16x4 lo[3], hi[3]; };

template <int NMM>
DI void p1_read(Frag& F, int idx, unsigned ax0, unsigned ax1, const unsigned (&aw)[4], int s) {
  if (idx == 0) F.x[0] = lds_b128(ax0);
  else if (idx == 3) F.x[1] = lds_b128(ax1);
  else if (idx < (NMM > 6 ? 10 : 8)) {
    const int k = idx < 3 ? idx - 1 : idx - 2;
    if ((k & 1) == 0) F.lo[k >> 1] = lds_tr(aw[k >> 1] + W_OFF + s * 8192);
    else F.hi[k >> 1] = lds_tr(aw[k >> 1] + W_OFF + s * 8192 + 2048);
  }
}

template <int NMM, int NH>
DI void p1_compute(f32x16 (&acc)[2][4], f32x4 (&ah)[3], unsigned smb, unsigned lx0, unsigned lx1, const unsigned (&lw)[4],
                   unsigned lxh, const unsigned (&lh)[3] TSARG) {
  Frag F[2];
  HFrag H;
  unsigned aw[4];
#pragma unroll
  for (int n = 0; n < 4; ++n) aw[n] = smb + lw[n];
  WAIT_VMI(NH ? 0 : (NMM > 6 ? 4 : 3));
  BARRIER_RAW();
  ST(1);
#pragma unroll
  for (int i = 0; i < 10; ++i) p1_read<NMM>(F[0], i, smb + lx0, smb + lx1, aw, 0);
  unsigned cur = smb;
#pragma unroll 1
  for (int t = 0; t < 10; ++t) {
    const unsigned nxt = (cur == smb + 2 * K1_STAGE) ? smb : cur + K1_STAGE;
#pragma unroll
    for (int s = 0; s < 4; ++s) {
      const Frag& C = F[s & 1];
      Frag& N = F[(s + 1) & 1];
      const unsigned nb = s == 3 ? nxt : cur;
      const int ns = (s + 1) & 3;
      const unsigned ax0 = nb + (lx0 ^ (32 * ns)), ax1 = nb + (lx1 ^ (32 * ns));
#pragma unroll
      for (int mm = 0; mm < NMM; ++mm) {
        const int n = mm >> 1, m = mm & 1;
        acc[m][n] = MFMA32(cat(C.lo[n], C.hi[n]), C.x[m], acc[m][n]);
        if (s == 3 && mm == 0) {
          SCHED();
          BARRIER();
#pragma unroll
          for (int q = 0; q < 4; ++q) aw[q] = nxt + lw[q];
        }
        p1_read<NMM>(N, 2 * mm, ax0, ax1, aw, ns); p1_read<NMM>(N, 2 * mm + 1, ax0, ax1, aw, ns);
        if (NH) {
          if (mm == 4 && (s == 1 || s == 2)) {
#pragma unroll
            for (int j = 0; j < NH; ++j) ah[j] = MFMA16(cat(H.lo[j], H.hi[j]), H.x, ah[j]);
          }
          if (mm >= 5 && s < 2) {
            if (mm == 5) H.x = lds_b128(cur + (lxh ^ (64 * s)));
            if (mm - 5 < NH) { H.lo[mm - 5] = lds_tr(cur + lh[mm - 5] + W_OFF + s * 16384); H.hi[mm - 5] = lds_tr(cur + lh[mm - 5] + W_OFF + s * 16384 + 2048); }
          }
        }
        SCHED();
      }
    }
    cur = nxt;
  }
  ST(2);
}

DI unsigned p1_lx(int m32, int lane) {
  const int r = lane & 31;
  return (16 + 32 * m32 + r) * 128 + ((((lane >> 5)) ^ ((r >> 1) & 7)) * 16);
}
DI unsigned p1_lw(int T, int lane) {
  const int h = lane >> 5, q = (lane >> 2) & 3, p = lane & 3, cb = (lane >> 4) & 1;
  return (8 * h + q) * 512 + ((64 * T + 32 * cb + 8 * p) ^ ((q << 6) | (h << 5)));
}

template <int NMM>
DI void p1_mlp(unsigned smb, int w, int lane, int f0, int rt, const u16* __restrict__ b1, rsrc_t ws TSARG) {
  const int h = lane >> 5;
  const int nh = w >> 1;
  const int ma = 2 * (w & 1) + nh, mb = ma ^ 1;
  const int T[4] = {3 * nh, 3 * nh + 1, 3 * nh + 2, 6};
  f32x16 acc[2][4]; f32x4 ah[3];
  unsigned lw[4];
  unsigned bv[4];
#pragma unroll
  for (int n = 0; n < 4; ++n) {
    lw[n] = p1_lw(T[n], lane);
    if (n < 3 || NMM > 6)
      asm volatile("global_load_ushort %0, %1, %2" : "=v"(bv[n]) : "v"((unsigned)((f0 + 32 * T[n] + (lane & 31)) * 2)), "s"(b1) : "memory");
    acc[0][n] = f32x16{}; acc[1][n] = f32x16{};
    asm volatile("" : "+v"(acc[0][n]), "+v"(acc[1][n]));
  }
  const unsigned lh[3] = {0u, 0u, 0u};
  p1_compute<NMM, 0>(acc, ah, smb, p1_lx(ma, lane), p1_lx(mb, lane), lw, 0u, lh TSPASS);
  {
    WAIT_VM(0);
    const u32x4 one = {h ? 0u : 0x3f80u, 0u, 0u, 0u};
#pragma unroll
    for (int mm = 0; mm < NMM; ++mm) {
      const int n = mm >> 1, m = mm & 1;
      asm volatile("" : "+v"(bv[n]));
      const u32x4 bf = {h ? 0u : bv[n], 0u, 0u, 0u};
      acc[m][n] = MFMA32(__builtin_bit_cast(bf16x8, bf), __builtin_bit_cast(bf16x8, one), acc[m][n]);
    }
  }
  asm volatile("s_nop 15\n\ts_nop 15" : "+v"(acc[0][0]), "+v"(acc[1][0]), "+v"(acc[0][1]), "+v"(acc[1][1]), "+v"(acc[0][2]), "+v"(acc[1][2]));
  if (NMM > 6) asm volatile("" : "+v"(acc[0][3]));
#pragma unroll
  for (int mm = 0; mm < NMM; ++mm) {
    const int n = mm >> 1, m = mm & 1;
    const unsigned dst = ((40 + (f0 >> 4) + 2 * T[n]) * 64 + rt * 4 + (m ? mb : ma)) * 1024 + lane * 16;
#pragma unroll
    for (int u = 0; u < 2; ++u) {
      const u32x4 o = mish8(acc[m][n], 8 * u);
      st128_sc1(ws, dst + u * 65536, o);
    }
  }
}

constexpr int XMT = 7680, XDUMP = 9 * XMT;
DI unsigned xslot_off(int sg, int odd) {
  int part, beta; qkv_slot(sg, odd, part, beta);
  return part * 2560 + beta * 512;
}

template <int NH>
DI void p1_qkv(unsigned smb, int w, int lane, int odd TSARG) {
  const int h = lane >> 5, g = lane >> 4, i = lane & 15;
  const int ma = 2 * (w & 1), T0 = 4 * (w >> 1);
  f32x16 acc[2][4] = {}; f32x4 ah[3] = {};
  const unsigned lxh = i * 128 + ((g ^ (i >> 1)) * 16);
  const int q = (lane >> 2) & 3, p = lane & 3;
  const unsigned hrow = (8 * g + q) * 512, hsw = (q << 6) | ((g & 1) << 5);
  const int hs[3] = {2 * w, 2 * w + 1, 12 + (w & 1)};
  unsigned lh[3], lw[4];
#pragma unroll
  for (int j = 0; j < 3; ++j) lh[j] = hrow + ((32 * hs[j] + 8 * p) ^ hsw);
#pragma unroll
  for (int n = 0; n < 4; ++n) { lw[n] = p1_lw(T0 + n, lane); asm volatile("" : "+v"(acc[0][n]), "+v"(acc[1][n])); }
  p1_compute<8, NH>(acc, ah, smb, p1_lx(ma, lane), p1_lx(ma + 1, lane), lw, lxh, lh TSPASS);
  const unsigned lb = smb + (1 + 2 * ma + (g & 1)) * XMT + h * 128 + i * 8;
#pragma unroll
  for (int n = 0; n < 4; ++n)
#pragma unroll
    for (int a2 = 0; a2 < 2; ++a2) {
      const int sg = 2 * (T0 + n) + a2;
      if (n == 3 && a2 == 1 && sg == 15) continue;
      const unsigned xo = lb + xslot_off(sg, odd);
#pragma unroll
      for (int a1 = 0; a1 < 2; ++a1)
#pragma unroll
        for (int m = 0; m < 2; ++m) {
          const int a = 2 * a2 + a1;
          lds_w64(xo + a1 * 256 + m * 2 * XMT, u32x2{pack_bf16(acc[m][n][4 * a], acc[m][n][4 * a + 1]), pack_bf16(acc[m][n][4 * a + 2], acc[m][n][4 * a + 3])});
        }
    }
#pragma unroll
  for (int j = 0; j < NH; ++j)
    lds_w64(smb + xslot_off(hs[j], odd) + lane * 8, u32x2{pack_bf16(ah[j][0], ah[j][1]), pack_bf16(ah[j][2], ah[j][3])});
}

DI void p1_attn(unsigned smb, int w, int lane, int rt, int hg, rsrc_t ws) {
  const int g = lane >> 4, i = lane & 15;
  const unsigned base = smb + w * XMT;
  const unsigned pa = base + lane * 8, pe = base + (5 * g + 4) * 128 + i * 8;
  const unsigned va = base + 2560 + (lane & 3) * 128 + (lane >> 2) * 8, ve = base + 2560 + (5 * (lane & 3) + 4) * 128 + (lane >> 2) * 8;
  u32x2 q[5], kp[5], kc[5], vp[5], vc[5];
#pragma unroll
  for (int t = 0; t < 5; ++t) {
    const unsigned a = t < 4 ? pa + t * 640 : pe;
    q[t] = lds_b64(a + XMT + 5120);
    kp[t] = lds_b64(a); kc[t] = lds_b64(a + XMT);
  }
#pragma unroll
  for (int t = 0; t < 5; ++t) {
    const unsigned a = t < 4 ? va + t * 640 : ve;
    vp[t] = __builtin_bit_cast(u32x2, lds_tr(a)); vc[t] = __builtin_bit_cast(u32x2, lds_tr(a + XMT));
  }
  const int par = i & 1;
  const bool c0 = (4 * g + par) <= i, c1 = (4 * g + par + 2) <= i;
  const bool first = (rt == 0) && (w == 0);
  const unsigned hm = par ? 0xffff0000u : 0x0000ffffu;
  const unsigned m_p0 = c0 ? 0u : hm, m_c0 = c0 ? hm : 0u, m_p1 = c1 ? 0u : hm, m_c1 = c1 ? hm : 0u;
  const float cs = 0.22360679774997896f * 1.44269504088896f;
  const f32x4 z4 = {0.f, 0.f, 0.f, 0.f};
  f32x4 oe = z4;
  float inv_e = 0.f;
  u32x2 op[4];
#pragma unroll
  for (int j = 0; j < 4; ++j) {
    u32x2 qe = q[4]; if (g != j) qe = u32x2{0u, 0u};
    bf16x8 qop = mk8(q[j], qe);
    f32x4 sp = MFMA16(mk8(kp[j], kp[4]), qop, z4);
    f32x4 sc = MFMA16(mk8(kc[j], kc[4]), qop, z4);
    float a0 = par ? sc[1] : sc[0], a1 = par ? sc[3] : sc[2];
    float b0 = par ? sp[1] : sp[0], b1 = par ? sp[3] : sp[2];
    if (first) { b0 = -INFINITY; b1 = -INFINITY; }
    float s0 = c0 ? a0 : b0, s1 = c1 ? a1 : b1;
    float mx = xg_max(fmaxf(s0, s1));
    float nm = -mx * cs;
    float p0 = __builtin_amdgcn_exp2f(fmaf(s0, cs, nm)), p1 = __builtin_amdgcn_exp2f(fmaf(s1, cs, nm));
    unsigned w0 = pack_bf16(p0, p0), w1 = pack_bf16(p1, p1);
    float sum = xg_sum(bf_lo(w0) + bf_lo(w1));
    float inv = __builtin_amdgcn_rcpf(sum);
    u32x4 pw = {w0 & m_p0, w1 & m_p1, w0 & m_c0, w1 & m_c1};
    bf16x8 pop = __builtin_bit_cast(bf16x8, pw);
    f32x4 o = MFMA16(mk8(vp[j], vc[j]), pop, z4);
    u32x2 ep = vp[4], ec = vc[4];
    if ((i >> 2) != j) { ep = u32x2{0u, 0u}; ec = u32x2{0u, 0u}; }
    oe = MFMA16(mk8(ep, ec), pop, oe);
    if (g == j) inv_e = inv;
    op[j] = u32x2{pack_bf16(o[0] * inv, o[1] * inv), pack_bf16(o[2] * inv, o[3] * inv)};
  }
  const int m32 = rt * 4 + (w >> 1);
  const unsigned lp = (32 * (g & 1) + 16 * (w & 1) + i) * 16;
  const unsigned d0 = ((5 * hg + (g >> 1)) * 64 + m32) * 1024 + lp;
  st128_sc1(ws, d0, u32x4{op[0][0], op[0][1], op[1][0], op[1][1]});
  st128_sc1(ws, d0 + 2 * 65536, u32x4{op[2][0], op[2][1], op[3][0], op[3][1]});
  st64_sc1(ws, ((5 * hg + 4) * 64 + m32) * 1024 + lp + (g >> 1) * 8,
           u32x2{pack_bf16(oe[0] * inv_e, oe[1] * inv_e), pack_bf16(oe[2] * inv_e, oe[3] * inv_e)});
}

template <int NM> struct Frag2 { bf16x8 a[2][NM]; s16x4 lo[2], hi[2]; };
template <int NM>
DI void p2_stage(const Frag2<NM>& C, Frag2<NM>& N, unsigned pa, unsigned pw, f32x16 (&acc)[NM]) {
#pragma unroll
  for (int k = 0; k < 2; ++k) {
#pragma unroll
    for (int m = 0; m < NM; ++m) {
      acc[m] = MFMA32(cat(C.lo[k], C.hi[k]), C.a[k][m], acc[m]);
      if (m == 0) { N.lo[k] = lds_tr(pw + k * 2048); N.hi[k] = lds_tr(pw + k * 2048 + 1024); }
      N.a[k][m] = lds_b128(pa + (k * NM + m) * 1024);
      SCHED();
    }
  }
}
constexpr int RED_OFF = 16384;
template <int NM>
DI void p2_compute(unsigned smb, int w, int lane, int m0, GAS float* gate TSARG) {
  const int kh = w >> 1, nh = w & 1, h = lane >> 5;
  constexpr int D = Ring<NM>::D, A_SLOT = Ring<NM>::A_SLOT;
  const unsigned la = smb + kh * (2 * NM * 1024) + lane * 16;
  const int q = (lane >> 2) & 3;
  const unsigned lw = smb + Ring<NM>::W_RING + kh * 4096 + (4 * h + q) * 128 + ((64 * nh + 32 * ((lane >> 4) & 1) + 8 * (lane & 3)) ^ ((q >> 1) << 6));
  f32x16 accA[NM] = {}, accB[NM] = {};
  Frag2<NM> F0, F1;
  WAIT_VM(0);
  BARRIER_RAW();
  ST(7);
  float gv[NM];
#pragma unroll
  for (int m = 0; m < NM; ++m) gv[m] = __hip_atomic_load(gate + (m0 + m) * 32 + (lane & 31), RLX_AGENT);
#pragma unroll
  for (int k = 0; k < 2; ++k) {
    F0.lo[k] = lds_tr(lw + k * 2048); F0.hi[k] = lds_tr(lw + k * 2048 + 1024);
#pragma unroll
    for (int m = 0; m < NM; ++m) F0.a[k][m] = lds_b128(la + (k * NM + m) * 1024);
  }
  unsigned ca = A_SLOT, cw = W_SLOT;
  auto adv = [&]() { ca = (ca == (D - 1) * A_SLOT) ? 0 : ca + A_SLOT; cw = (cw == (D - 1) * W_SLOT) ? 0 : cw + W_SLOT; };
#pragma unroll 1
  for (int t = 0; t < 26; t += 2) {
    BARRIER_RAW(); p2_stage<NM>(F0, F1, la + ca, lw + cw, accB); adv();
    BARRIER_RAW(); p2_stage<NM>(F1, F0, la + ca, lw + cw, accB); adv();
  }
  BARRIER_RAW(); p2_stage<NM>(F0, F1, la + ca, lw + cw, accB); adv();
  ST(8);
  BARRIER_RAW(); p2_stage<NM>(F1, F0, la + ca, lw + cw, accA); adv();
#pragma unroll 1
  for (int t = 28; t < NST - 1; t += 2) {
    BARRIER_RAW(); p2_stage<NM>(F0, F1, la + ca, lw + cw, accA); adv();
    BARRIER_RAW(); p2_stage<NM>(F1, F0, la + ca, lw + cw, accA); adv();
  }
#pragma unroll
  for (int k = 0; k < 2; ++k)
#pragma unroll
    for (int m = 0; m < NM; ++m) accA[m] = MFMA32(cat(F0.lo[k], F0.hi[k]), F0.a[k][m], accA[m]);
  ST(9);
  BARRIER();
  f32x4 keep[NM][2];
#pragma unroll
  for (int m = 0; m < NM; ++m) {
    const float hv = 1.f - gv[m];
#pragma unroll
    for (int a = 0; a < 2; ++a) {
      f32x4 lo, hi;
#pragma unroll
      for (int e = 0; e < 4; ++e) {
        lo[e] = gv[m] * accA[m][4 * a + e] + hv * accB[m][4 * a + e];
        hi[e] = gv[m] * accA[m][8 + 4 * a + e] + hv * accB[m][8 + 4 * a + e];
      }
      keep[m][a] = kh ? hi : lo;
      *(LAS f32x4*)(size_t)(smb + RED_OFF + ((w * NM + m) * 2 + a) * 1024 + lane * 16) = kh ? lo : hi;
    }
  }
  BARRIER();
#pragma unroll
  for (int m = 0; m < NM; ++m)
#pragma unroll
    for (int a = 0; a < 2; ++a) {
      const f32x4 o = keep[m][a] + *(LAS f32x4*)(size_t)(smb + RED_OFF + (((w ^ 2) * NM + m) * 2 + a) * 1024 + lane * 16);
      lds_w64(smb + (m * 32 + (lane & 31)) * 144 + nh * 64 + (2 * kh + a) * 16 + h * 8, u32x2{pack_bf16(o[0], o[1]), pack_bf16(o[2], o[3])});
    }
}

__global__ __launch_bounds__(NTHREADS, 2) void fused_kernel(const u16* __restrict__ x, const u16* __restrict__ wqkv, const u16* __restrict__ wo,
                                                            const u16* __restrict__ w1, const u16* __restrict__ b1, const u16* __restrict__ w2,
                                                            const u16* __restrict__ gw, char* __restrict__ wsp, u16* __restrict__ out) {
  extern __shared__ __attribute__((aligned(16))) char smem[];
  const unsigned smb = (unsigned)(size_t)(LAS char*)smem;
  TSDECL; ST(0);
  const int lane = threadIdx.x & 63;
  const int w = __builtin_amdgcn_readfirstlane(threadIdx.x >> 6);
  int b = blockIdx.x;
  const int is_mlp = (b >> 3) & 1;
  const int rt = ((b >> 1) & 3) * 4 + ((b >> 4) & 3);
  const int cg = (b & 1) * 4 + (b >> 6);
  const int row0 = rt * 128;
  const int ps = cg < 7 ? 7 * cg : 48, np = cg < 6 ? 7 : 6;
  int k0 = ((5 * (rt & 3)) >> 1) + ((5 * (is_mlp * 4 + (cg & 3))) >> 2);
  k0 = ROT1 == 2 ? (k0 >= 9 ? k0 - 8 : k0 + 1) : (ROT1 ? (k0 >= 10 ? k0 - 10 : k0) : 0);
  const char* wmat = is_mlp ? (const char*)w1 : (const char*)wqkv;
  const unsigned vwme = p1_vw(w, lane, is_mlp, cg, ps, np);
  {
    const int kf = ROT1 == 2 ? 0 : k0;
    const unsigned RB = is_mlp ? 3456u : 3840u;
    const char* xs = (const char*)x + kf * 128;
    const char* wsrc = wmat + kf * (64 * RB);
    const unsigned d = smb + w * 1024;
    const unsigned vx = (row0 + 8 * w + (lane >> 3)) * 1280 + (((lane & 7) ^ (4 * (w & 1) + (lane >> 4))) * 16);
    glds16(xs, vx, d + 2048);
    glds16(wsrc, vwme, d + W_OFF);
    glds16(xs + 64 * 1280, vx, d + 2048 + 8192);
    glds16(wsrc + 16 * RB, vwme, d + W_OFF + 8192);
    glds16(wsrc + 32 * RB, vwme, d + W_OFF + 16384);
    glds16(wsrc + 48 * RB, vwme, d + W_OFF + 24576);
    int tokh = row0 - 16 + 2 * w + (lane >> 5); tokh = tokh < 0 ? 0 : tokh;
    const unsigned vh = tokh * 1280 + (((((lane & 31) >> 2) ^ w) * 16) | ((lane & 3) * 4));
    asm volatile("s_mov_b32 m0, %2\n\ts_nop 0\n\tglobal_load_lds_dword %0, %1" : : "v"(vh), "s"(xs), "s"(smb + w * 256) : "memory");
  }
  if (ROT1 != 2) k0 = p1_next(k0);
  if (w >= 4) {
    {
      const char* pc;
      asm volatile("s_getpc_b64 %0" : "=s"(pc));
      const int chunk = ((b >> 3) * 4 + w) % CODE_CHUNKS;
      asm volatile("s_mov_b32 m0, %2\n\ts_mov_b32 exec_hi, 0\n\tglobal_load_lds_dword %0, %1\n\ts_mov_b32 exec_hi, -1"
                   : : "v"((unsigned)(chunk * 4096 + lane * 128)), "s"(pc), "s"(smb + 3 * K1_STAGE + w * 256) : "memory");
    }
    const unsigned vw0 = p1_vw(w - 4, lane, is_mlp, cg, ps, np);
    if (is_mlp) p1_loader<0>(smb, w - 4, lane, (const char*)x, wmat, row0, vw0, vwme, k0 TSPASS);
    else p1_loader<1>(smb, w - 4, lane, (const char*)x, wmat, row0, vw0, vwme, k0 TSPASS);
  }
  asm volatile("" : "+s"(b));
  const int xcd = b & 7, idx = b >> 3;
  int ct, m0;
  {
    const int j = idx >> 1, jj = j - 6;
    const int ctm = j < 12 ? j >> 2 : 3 + ((j - 12) >> 1), m0m = 3 * (j < 12 ? j & 3 : j & 1);
    const int ctq = j < 6 ? j >> 1 : 3 + (jj >= 5), m0q = j < 6 ? 12 + 2 * (j & 1) : 6 + 2 * (jj >= 5 ? jj - 5 : jj);
    ct = is_mlp ? ctm : ctq; m0 = is_mlp ? m0m : m0q;
  }
  int s0 = ROT2 ? ((m0 * 27) >> 4) + ct * 5 : 0;
  s0 = s0 >= 27 ? s0 - 27 : s0;
  ct += (xcd & 1) * 5; m0 += (xcd >> 1) * 16;
  const int nm = is_mlp ? 3 : 2;
  const rsrc_t ws = __builtin_amdgcn_make_buffer_rsrc(wsp, 0, WS_BYTES, 0x00020000);
  GAS u64* flags = (GAS u64*)(wsp + FLAG_OFF);
  GAS u64* epoch = (GAS u64*)(wsp + EPOCH_OFF);
  GAS float* gate = (GAS float*)(wsp + GATE_OFF);
  WLoad wl = {0u, 0u, nullptr, 0};
  u64 tag = 0;
  if (w >= 4) {
    wl = is_mlp ? p2_w_prologue<3>(smb, w - 4, lane, ct, s0, w2) : p2_w_prologue<2>(smb, w - 4, lane, ct, s0, w2);
    if (w < 6) {
      const int id = idx * 100 + (w - 4) * 50 + (lane < 50 ? lane : 49);
      const int row = id / 5;
      unsigned dm;
      asm volatile("global_load_dword %0, %1, %2" : "=v"(dm) : "v"((unsigned)(row * 1280 + ((xcd & 1) * 5 + (id - row * 5)) * 128)), "s"(wo) : "memory");
      BARRIER_RAW();
      asm volatile("s_waitcnt vmcnt(0)" : "+v"(dm) :: "memory");
    } else
    BARRIER_RAW();
    tag = __hip_atomic_load(epoch, RLX_AGENT) * 0x9E3779B97F4A7C15ull + 0x632BE59BD9B4E019ull;
    if (w >= 6) {
      if (is_mlp) {
        const int row = row0 + cg * 16 + (w - 6) * 8 + (lane >> 3);
        const char* xr = (const char*)x + row * 1280 + (lane & 7) * 16;
        const char* gr = (const char*)gw + (lane & 7) * 16;
        float s = 0.f;
#pragma unroll
        for (int t = 0; t < 10; ++t) {
          u32x4 a = *(const u32x4*)(xr + t * 128), c = *(const u32x4*)(gr + t * 128);
#pragma unroll
          for (int e = 0; e < 4; ++e) {
            const unsigned ua = a[e], uc = c[e];
            s = __builtin_amdgcn_fdot2_f32_bf16(__builtin_bit_cast(bf16x2_t, ua), __builtin_bit_cast(bf16x2_t, uc), s, false);
          }
        }
        s += __shfl_xor(s, 1); s += __shfl_xor(s, 2); s += __shfl_xor(s, 4);
        if ((lane & 7) == 0) __hip_atomic_store(gate + row, 1.f / (1.f + __expf(-s)), RLX_AGENT);
      }
    }
    ST(1); ST(2);
  } else if (is_mlp) {
    const int f0 = ps * 32;
    if (np == 7) p1_mlp<7>(smb, w, lane, f0, rt, b1, ws TSPASS);
    else p1_mlp<6>(smb, w, lane, f0, rt, b1, ws TSPASS);
  } else {
    if (w < 2) p1_qkv<3>(smb, w, lane, cg & 1 TSPASS);
    else p1_qkv<2>(smb, w, lane, cg & 1 TSPASS);
  }
  if (!is_mlp) {
    BARRIER();
    ST(3);
    p1_attn(smb, w, lane, rt, cg, ws);
  } else { ST(3); }
  ST(4);
  GAS u64* myflag = flags + rt * 16 + is_mlp * 8 + cg;
  if (is_mlp) WAIT_VM(0);
  BARRIER();
  ST(5);
  if (is_mlp && w == 6 && lane == 0) __hip_atomic_store(myflag, tag, RLX_AGENT);
  ST(6);
  if (w >= 4) {
    const int rta = m0 >> 2, rtb = (m0 + nm - 1) >> 2;
    GAS u64* fq = flags + ((lane & 8) ? rtb : rta) * 16 + (lane & 7);
    if (is_mlp) p2_loader<3>(smb, w - 4, lane, m0, s0, wl, wsp, wo, fq + 8, fq, tag, myflag);
    else p2_loader<2>(smb, w - 4, lane, m0, s0, wl, wsp, wo, fq + 8, fq, tag, myflag);
    ST(7); ST(8); ST(9);
    BARRIER_RAW();
    BARRIER_RAW();
  } else if (is_mlp) p2_compute<3>(smb, w, lane, m0, gate TSPASS);
  else p2_compute<2>(smb, w, lane, m0, gate TSPASS);
  BARRIER();
  for (int c = threadIdx.x; c < nm * 256; c += NTHREADS) {
    const int r = c >> 3, ch = c & 7;
    u32x4 v = *(LAS u32x4*)(size_t)(smb + r * 144 + ch * 16);
    *(u32x4*)((char*)out + (size_t)(m0 * 32 + r) * 1280 + ct * 128 + ch * 16) = v;
  }
  if (b == 254 && w == 7) {
    for (int spin = 0; spin < SPIN_CAP; ++spin) {
      bool ok = true;
#pragma unroll
      for (int k = 0; k < 4; ++k) ok &= __hip_atomic_load(flags + k * 64 + lane, RLX_AGENT) == tag;
      if (__all(ok)) break;
      __builtin_amdgcn_s_sleep(8);
    }
    if (lane == 0) __hip_atomic_store(epoch, tag, RLX_AGENT);
  }
}

extern "C" void kernel_launch(void* const* d_in, const int* in_sizes, int n_in, void* d_out, int out_size, void* d_ws,
                              size_t ws_size, hipStream_t stream) {
  (void)hipFuncSetAttribute((const void*)fused_kernel, hipFuncAttributeMaxDynamicSharedMemorySize, LDS_BYTES);
  fused_kernel<<<256, NTHREADS, LDS_BYTES, stream>>>((const u16*)d_in[0], (const u16*)d_in[1], (const u16*)d_in[2], (const u16*)d_in[4],
                                                     (const u16*)d_in[5], (const u16*)d_in[6], (const u16*)d_in[7], (char*)d_ws, (u16*)d_out);
}
```

```cpp
#define PROBE 1
#define PROBECLS 0
#include <hip/hip_runtime.h>
#include <stdint.h>
#include <type_traits>

typedef unsigned short u16;
typedef unsigned long long u64;
typedef short bf16x8 __attribute__((ext_vector_type(8)));
typedef short s16x4 __attribute__((ext_vector_type(4)));
typedef float f32x4 __attribute__((ext_vector_type(4)));
typedef float f32x16 __attribute__((ext_vector_type(16)));
typedef float f32x2 __attribute__((ext_vector_type(2)));
typedef unsigned u32x4 __attribute__((ext_vector_type(4)));
typedef unsigned u32x2 __attribute__((ext_vector_type(2)));
typedef unsigned u32x16 __attribute__((ext_vector_type(16)));
typedef __bf16 bf16x2_t __attribute__((ext_vector_type(2)));
#define LAS __attribute__((address_space(3)))
#define GAS __attribute__((address_space(1)))
#define DI __device__ __forceinline__

#define MFMA16(a, b, c) __builtin_amdgcn_mfma_f32_16x16x32_bf16((a), (b), (c), 0, 0, 0)
#define MFMA32(a, b, c) __builtin_amdgcn_mfma_f32_32x32x16_bf16((a), (b), (c), 0, 0, 0)
#define WAIT_VM(n) asm volatile("s_waitcnt vmcnt(" #n ")" ::: "memory")
#define WAIT_VMI(n) asm volatile("s_waitcnt vmcnt(%0)" ::"i"(n) : "memory")
#define BARRIER() do { asm volatile("s_waitcnt lgkmcnt(0)" ::: "memory"); __builtin_amdgcn_s_barrier(); asm volatile("" ::: "memory"); } while (0)
#define BARRIER_RAW() do { asm volatile("" ::: "memory"); __builtin_amdgcn_s_barrier(); asm volatile("" ::: "memory"); } while (0)
#define SCHED() __builtin_amdgcn_sched_barrier(0)
#define RLX_AGENT __ATOMIC_RELAXED, __HIP_MEMORY_SCOPE_AGENT

struct TS { unsigned long long t0, tp; };
#define TSARG , TS& ts
#define TSPASS , ts
#define TSDECL TS ts = {}
#define ST(id) do { if (id == 0) { ts.t0 = __builtin_amdgcn_s_memrealtime(); asm volatile("s_waitcnt lgkmcnt(0)" ::: "memory"); } if (id == PROBE) { ts.tp = __builtin_amdgcn_s_memrealtime(); asm volatile("s_waitcnt lgkmcnt(0)" ::: "memory"); } } while (0)
#define TSOUT()

constexpr int NTHREADS = 512;
constexpr int NKAP = 148;
constexpr int W_OFF = 18432;
constexpr int K1_STAGE = 51200;
constexpr unsigned MID_BYTES = NKAP * 64 * 1024;
constexpr unsigned GATE_OFF = MID_BYTES, FLAG_OFF = GATE_OFF + 8192, EPOCH_OFF = FLAG_OFF + 2048, WS_BYTES = EPOCH_OFF + 128;
constexpr int LDS_BYTES = 163840;
constexpr int W_SLOT = 8192;
template <int NM> struct Ring {
  static constexpr int D = NM == 3 ? 8 : 10;
  static constexpr int A_SLOT = NM * 4096;
  static constexpr int W_RING = D * A_SLOT;
};
constexpr int NST = NKAP / 4;
constexpr int SPIN_CAP = 1 << 22;
constexpr int PFN = 1;
constexpr int CODE_CHUNKS = 11;

DI unsigned pack_bf16(float lo, float hi) {
  f32x2 f = {lo, hi};
  return __builtin_bit_cast(unsigned, __builtin_convertvector(f, bf16x2_t));
}
DI float bf_lo(unsigned u) { return __uint_as_float(u << 16); }
DI float bf_hi(unsigned u) { return __uint_as_float(u & 0xffff0000u); }
DI void glds16(const char* sbase, unsigned voff, unsigned dst) {
  asm volatile("s_mov_b32 m0, %2\n\ts_nop 0\n\tglobal_load_lds_dwordx4 %0, %1" : : "v"(voff), "s"(sbase), "s"(dst) : "memory");
}
DI void glds16_sc1(const char* sbase, unsigned voff, unsigned dst) {
  asm volatile("s_mov_b32 m0, %2\n\ts_nop 0\n\tglobal_load_lds_dwordx4 %0, %1 sc1" : : "v"(voff), "s"(sbase), "s"(dst) : "memory");
}
DI void glds16_lo(const char* sbase, unsigned voff, unsigned dst) {
  asm volatile("s_mov_b32 m0, %2\n\ts_mov_b32 exec_hi, 0\n\tglobal_load_lds_dwordx4 %0, %1\n\ts_mov_b32 exec_hi, -1" : : "v"(voff), "s"(sbase), "s"(dst) : "memory");
}
DI bf16x8 lds_b128(unsigned a) { return *(LAS bf16x8*)(size_t)a; }
DI s16x4 lds_tr(unsigned a) { return __builtin_amdgcn_ds_read_tr16_b64_v4i16((LAS s16x4*)(size_t)a); }
DI u32x2 lds_b64(unsigned a) { return *(LAS u32x2*)(size_t)a; }
DI void lds_w64(unsigned a, u32x2 v) { *(LAS u32x2*)(size_t)a = v; }
DI bf16x8 cat(s16x4 lo, s16x4 hi) { return __builtin_shufflevector(lo, hi, 0, 1, 2, 3, 4, 5, 6, 7); }
DI bf16x8 mk8(u32x2 a, u32x2 b) {
  u32x4 t = {a[0], a[1], b[0], b[1]};
  return __builtin_bit_cast(bf16x8, t);
}
typedef __amdgpu_buffer_rsrc_t rsrc_t;
#define STAUX 16
DI void st128_sc1(rsrc_t r, unsigned off, u32x4 v) { __builtin_amdgcn_raw_buffer_store_b128(v, r, off, 0, STAUX); }
DI void st64_sc1(rsrc_t r, unsigned off, u32x2 v) { __builtin_amdgcn_raw_buffer_store_b64(v, r, off, 0, 16); }

DI u32x4 mish8(const f32x16& v, int o) {
  f32x2 h[4], z[4], e[4], p[4], d[4], r[4];
  const f32x2 c1 = {1.44269504088896f / 160.f, 1.44269504088896f / 160.f}, c0 = {129.f / 160.f, 129.f / 160.f};
#pragma unroll
  for (int k = 0; k < 4; ++k) { h[k] = f32x2{v[o + 2 * k], v[o + 2 * k + 1]}; asm("v_pk_fma_f32 %0, %1, %2, %3 clamp" : "=v"(z[k]) : "v"(h[k]), "v"(c1), "v"(c0)); }
#pragma unroll
  for (int k = 0; k < 4; ++k) z[k] = z[k] * 160.f - 129.f;
  SCHED();
#pragma unroll
  for (int k = 0; k < 4; ++k) e[k] = f32x2{__builtin_amdgcn_exp2f(z[k][0]), __builtin_amdgcn_exp2f(z[k][1])};
  SCHED();
#pragma unroll
  for (int k = 0; k < 4; ++k) p[k] = e[k] + 2.f;
#pragma unroll
  for (int k = 0; k < 4; ++k) d[k] = e[k] * p[k] + 2.f;
  SCHED();
#pragma unroll
  for (int k = 0; k < 4; ++k) r[k] = f32x2{__builtin_amdgcn_rcpf(d[k][0]), __builtin_amdgcn_rcpf(d[k][1])};
  SCHED();
#pragma unroll
  for (int k = 0; k < 4; ++k) h[k] = h[k] * e[k];
#pragma unroll
  for (int k = 0; k < 4; ++k) h[k] = h[k] * p[k];
#pragma unroll
  for (int k = 0; k < 4; ++k) h[k] = h[k] * r[k];
  u32x4 o4;
#pragma unroll
  for (int k = 0; k < 4; ++k) o4[k] = pack_bf16(h[k][0], h[k][1]);
  SCHED();
  return o4;
}
DI float xg_max(float v) {
  unsigned u = __float_as_uint(v);
  auto r = __builtin_amdgcn_permlane32_swap(u, u, false, false);
  v = fmaxf(__uint_as_float(r[0]), __uint_as_float(r[1]));
  u = __float_as_uint(v);
  auto r2 = __builtin_amdgcn_permlane16_swap(u, u, false, false);
  return fmaxf(__uint_as_float(r2[0]), __uint_as_float(r2[1]));
}
DI float xg_sum(float v) {
  unsigned u = __float_as_uint(v);
  auto r = __builtin_amdgcn_permlane32_swap(u, u, false, false);
  v = __uint_as_float(r[0]) + __uint_as_float(r[1]);
  u = __float_as_uint(v);
  auto r2 = __builtin_amdgcn_permlane16_swap(u, u, false, false);
  return __uint_as_float(r2[0]) + __uint_as_float(r2[1]);
}

#define RAMP0 2
template <int D>
constexpr int p2_issued(int t) {
  int n = RAMP0;
  for (int i = 0; i <= t; ++i) { n += 2; if (n > i + D - 1) n = i + D - 1; }
  return n > NST ? NST : n;
}
template <int NM, int D>
constexpr int p2_allowed(int t) {
  int n = 0;
  for (int st = t + 2; st < p2_issued<D>(t); ++st) n += st < D - 1 ? NM : NM + 2;
  return n;
}
template <int I, int N, class F>
DI void static_for(F&& f) {
  if constexpr (I < N) { f(std::integral_constant<int, I>{}); static_for<I + 1, N>(f); }
}
struct WLoad { unsigned voffn, voffe; const char* src; int st; };
template <int NM>
DI WLoad p2_w_prologue(unsigned smb, int l, int lane, int ct, int s0, const u16* __restrict__ w2) {
  const int r8 = lane >> 3;
  const unsigned colb = ct * 128 + (((lane & 7) ^ (((r8 >> 1) & 1) << 2)) * 16);
  WLoad s;
  s.voffn = r8 * 1280 + colb;
  s.voffe = (20 * (r8 >> 2) + (r8 & 3)) * 1280 + colb;
  s.src = (const char*)w2 + (16 * l) * 1280;
  const unsigned d = smb + Ring<NM>::W_RING + l * 2048;
  s.st = s0;
#pragma unroll 1
  for (int t = 0; t < Ring<NM>::D - 1; ++t) {
    const char* p = s.src + s.st * (64 * 1280);
    glds16(p, s.voffn, d + t * W_SLOT);
    glds16(p + 8 * 1280, s.voffn, d + t * W_SLOT + 1024);
    s.st = s.st == 26 ? 0 : s.st + 1;
  }
  return s;
}
DI void spin_flags(GAS u64* fp, u64 tag) {
#pragma unroll 1
  for (int spin = 0; spin < SPIN_CAP; ++spin) {
    if (__all(__hip_atomic_load(fp, RLX_AGENT) == tag)) break;
    __builtin_amdgcn_s_sleep(2);
  }
  asm volatile("" ::: "memory");
}
template <int NM>
DI void p2_loader(unsigned smb, int l, int lane, int m0, int sa, WLoad& ws, const char* __restrict__ mid, const u16* __restrict__ wo,
                  GAS u64* fpm, GAS u64* fpq, u64 tag, GAS u64* myflag) {
  constexpr int D = Ring<NM>::D, A_SLOT = Ring<NM>::A_SLOT;
  const unsigned lane16 = lane * 16;
  const char* src = mid + (size_t)((40 + l) * 64 + m0) * 1024;
  const unsigned da = smb + l * (NM * 1024), dw = smb + Ring<NM>::W_RING + l * 2048;
  auto issue_a = [&](unsigned slot) {
    const char* p = src + (size_t)sa * (4 * 65536);
#pragma unroll
    for (int m = 0; m < NM; ++m) glds16_sc1(p + m * 1024, lane16, da + slot * A_SLOT + m * 1024);
    sa = sa == 26 ? 0 : sa + 1;
  };
  spin_flags(fpm, tag);
#pragma unroll
  for (int t = 0; t < RAMP0; ++t) issue_a(t);
  WAIT_VMI((p2_allowed<NM, D>(-1)));
  BARRIER_RAW();
  if (NM == 2 && l == 2 && lane == 0) {
    asm volatile("global_store_dwordx2 %0, %1, off sc1" : : "v"(myflag), "v"(tag) : "memory");
  }
  unsigned ps = D - 1;
  u64 fq = 0;
  auto issue_act = [&]() {
    const char* p = ws.src + sa * (64 * 1280);
    glds16(p, ws.voffn, dw + ps * W_SLOT);
    glds16(p + 8 * 1280, ws.voffn, dw + ps * W_SLOT + 1024);
    issue_a(ps);
    ps = (ps == D - 1) ? 0 : ps + 1;
  };
  auto issue_range = [&](auto c0, auto c1) {
    static_for<c0.value, c1.value>([&](auto sc) { if constexpr (sc.value < D - 1) issue_a(sc.value); else issue_act(); });
  };
  issue_range(std::integral_constant<int, p2_issued<D>(-1)>{}, std::integral_constant<int, p2_issued<D>(0)>{});
  static_for<0, D - 3>([&](auto tc) {
    WAIT_VMI((p2_allowed<NM, D>(tc.value))); BARRIER_RAW();
    issue_range(std::integral_constant<int, p2_issued<D>(tc.value)>{}, std::integral_constant<int, p2_issued<D>(tc.value + 1)>{});
  });
  static_assert(p2_issued<D>(D - 3) == 2 * D - 4, "ring must be full when the steady state starts");
#pragma unroll 1
  for (int t = D - 3; t + D - 1 <= 24; ++t) { WAIT_VMI((NM + 2) * (D - 3)); BARRIER_RAW(); issue_act(); }
  asm volatile("global_load_dwordx2 %0, %1, off sc1" : "=v"(fq) : "v"(fpq) : "memory");
  WAIT_VMI((NM + 2) * (D - 3)); BARRIER_RAW(); issue_act();
  WAIT_VMI((NM + 2) * (D - 3)); BARRIER_RAW(); issue_act();
  asm volatile("s_waitcnt vmcnt(%1)" : "+v"(fq) : "i"(2 * (NM + 2)) : "memory");
  if (!__all(fq == tag)) spin_flags(fpq, tag);
  src = mid + (size_t)(l * 64 + m0) * 1024; sa = 0;
  int u = l;
  const char* won = (const char*)wo;
#pragma unroll 1
  for (int j = 0; j < 10; ++j) {
    WAIT_VMI((NM + 2) * (D - 3)); BARRIER_RAW();
    const int r0 = u < 4 ? 40 * (u >> 1) + 8 * (u & 1) : 16, dr = u < 4 ? 20 : 40;
    const unsigned vo = u < 4 ? ws.voffn : ws.voffe;
    glds16(won + r0 * 1280, vo, dw + ps * W_SLOT);
    glds16(won + (r0 + dr) * 1280, vo, dw + ps * W_SLOT + 1024);
    won += u >= 1 ? 80 * 1280 : 0;
    u = u >= 1 ? u - 1 : 4;
    issue_a(ps);
    ps = (ps == D - 1) ? 0 : ps + 1;
  }
  static_for<NST - D + 1, NST - 1>([&](auto tc) { WAIT_VMI((p2_allowed<NM, D>(tc.value))); BARRIER_RAW(); });
}

DI void qkv_slot(int sg, int odd, int& P, int& beta) {
  if (sg < 12) { P = sg >> 2; beta = (sg & 3) + odd; }
  else { P = sg < 15 ? sg - 12 : 2; beta = odd ? 0 : 4; }
}
DI unsigned p1_vw(int v, int lane, int is_mlp, int cg, int ps, int np) {
  const int hi = lane >> 5;
  const int c = (lane & 31) ^ ((((2 * v + hi) & 3) << 2) | ((v >> 2) << 1));
  const int sg = c >> 1, odd = cg & 1;
  const int P = sg < 12 ? sg >> 2 : (sg < 15 ? sg - 12 : 2);
  const int beta = sg < 12 ? (sg & 3) + odd : (odd ? 0 : 4);
  const unsigned colq = (P == 2 ? 0 : (P + 1) * 1280) + cg * 160 + beta * 32 + (c & 1) * 16;
  const unsigned colm = ps * 64 + (c < 4 * np ? c : 4 * np - 1) * 16;
  return (2 * v + hi) * (is_mlp ? 3456u : 3840u) + (is_mlp ? colm : colq);
}
#define ROT1 2
#define ROT2 1
#define PRE1 5
DI int p1_next(int kk) { return kk == 9 ? (ROT1 == 2 ? 1 : 0) : kk + 1; }
template <int QKV>
DI void p1_loader(unsigned smb, int L, int lane, const char* __restrict__ x, const char* __restrict__ wmat, int row0, unsigned vw0, unsigned vw1, int kk TSARG) {
  constexpr unsigned RB = QKV ? 3840 : 3456;
  constexpr int NL = QKV ? 13 : 12;
  const unsigned vx = (row0 + 8 * L + (lane >> 3)) * 1280 + (((lane & 7) ^ (4 * (L & 1) + (lane >> 4))) * 16);
  const int hr = 4 * L + ((lane >> 3) & 3);
  int tokh = row0 - 16 + hr; tokh = tokh < 0 ? 0 : tokh;
  const unsigned vh = tokh * 1280 + (((lane & 7) ^ ((hr >> 1) & 7)) * 16);
  auto piece = [&](int p, unsigned buf) {
    const unsigned d = smb + buf + L * 1024;
    const char* xs = x + kk * 128;
    const char* wsrc = wmat + kk * (64 * RB);
    if (p < 8) { if (p & 1) glds16(xs + (p >> 1) * (32 * 1280), vx, d + 2048 + (p >> 1) * 4096);
                 else glds16(wsrc + (p >> 2) * (16 * RB), (p & 2) ? vw1 : vw0, d + W_OFF + (p >> 1) * 4096); }
    else if (p < 12) glds16(wsrc + ((p - 4) >> 1) * (16 * RB), (p & 1) ? vw1 : vw0, d + W_OFF + (p - 4) * 4096);
    else glds16_lo(xs, vh, smb + buf + L * 512);
  };
  auto issue = [&](unsigned buf) {
#pragma unroll
    for (int p = 0; p < NL; ++p) piece(p, buf);
    kk = p1_next(kk);
  };
#pragma unroll
  for (int p = 0; p < PRE1; ++p) piece(p, K1_STAGE);
  WAIT_VMI(PRE1 + PFN);
  BARRIER_RAW();
#pragma unroll
  for (int p = PRE1; p < NL; ++p) piece(p, K1_STAGE);
  kk = p1_next(kk);
  issue(2 * K1_STAGE);
  unsigned nb = 0;
#pragma unroll 1
  for (int t = 0; t < 7; ++t) {
    WAIT_VMI(NL);
    BARRIER_RAW();
    issue(nb);
    nb = (nb == 2 * K1_STAGE) ? 0 : nb + K1_STAGE;
  }
  WAIT_VMI(NL);
  BARRIER_RAW();
  WAIT_VM(0);
  BARRIER_RAW();
}

struct Frag { bf16x8 x[2]; s16x4 lo[4], hi[4]; };
struct HFrag { bf16x8 x; s16x4 lo[3], hi[3]; };

template <int NMM>
DI void p1_read(Frag& F, int idx, unsigned ax0, unsigned ax1, const unsigned (&aw)[4], int s) {
  if (idx == 0) F.x[0] = lds_b128(ax0);
  else if (idx == 3) F.x[1] = lds_b128(ax1);
  else if (idx < (NMM > 6 ? 10 : 8)) {
    const int k = idx < 3 ? idx - 1 : idx - 2;
    if ((k & 1) == 0) F.lo[k >> 1] = lds_tr(aw[k >> 1] + W_OFF + s * 8192);
    else F.hi[k >> 1] = lds_tr(aw[k >> 1] + W_OFF + s * 8192 + 2048);
  }
}

template <int NMM, int NH>
DI void p1_compute(f32x16 (&acc)[2][4], f32x4 (&ah)[3], unsigned smb, unsigned lx0, unsigned lx1, const unsigned (&lw)[4],
                   unsigned lxh, const unsigned (&lh)[3] TSARG) {
  Frag F[2];
  HFrag H;
  unsigned aw[4];
#pragma unroll
  for (int n = 0; n < 4; ++n) aw[n] = smb + lw[n];
  WAIT_VMI(NH ? 0 : (NMM > 6 ? 4 : 3));
  BARRIER_RAW();
  ST(1);
#pragma unroll
  for (int i = 0; i < 10; ++i) p1_read<NMM>(F[0], i, smb + lx0, smb + lx1, aw, 0);
  unsigned cur = smb;
#pragma unroll 1
  for (int t = 0; t < 10; ++t) {
    const unsigned nxt = (cur == smb + 2 * K1_STAGE) ? smb : cur + K1_STAGE;
#pragma unroll
    for (int s = 0; s < 4; ++s) {
      const Frag& C = F[s & 1];
      Frag& N = F[(s + 1) & 1];
      const unsigned nb = s == 3 ? nxt : cur;
      const int ns = (s + 1) & 3;
      const unsigned ax0 = nb + (lx0 ^ (32 * ns)), ax1 = nb + (lx1 ^ (32 * ns));
#pragma unroll
      for (int mm = 0; mm < NMM; ++mm) {
        const int n = mm >> 1, m = mm & 1;
        acc[m][n] = MFMA32(cat(C.lo[n], C.hi[n]), C.x[m], acc[m][n]);
        if (s == 3 && mm == 0) {
          SCHED();
          BARRIER();
#pragma unroll
          for (int q = 0; q < 4; ++q) aw[q] = nxt + lw[q];
        }
        p1_read<NMM>(N, 2 * mm, ax0, ax1, aw, ns); p1_read<NMM>(N, 2 * mm + 1, ax0, ax1, aw, ns);
        if (NH) {
          if (mm == 4 && (s == 1 || s == 2)) {
#pragma unroll
            for (int j = 0; j < NH; ++j) ah[j] = MFMA16(cat(H.lo[j], H.hi[j]), H.x, ah[j]);
          }
          if (mm >= 5 && s < 2) {
            if (mm == 5) H.x = lds_b128(cur + (lxh ^ (64 * s)));
            if (mm - 5 < NH) { H.lo[mm - 5] = lds_tr(cur + lh[mm - 5] + W_OFF + s * 16384); H.hi[mm - 5] = lds_tr(cur + lh[mm - 5] + W_OFF + s * 16384 + 2048); }
          }
        }
        SCHED();
      }
    }
    cur = nxt;
  }
  ST(2);
}

DI unsigned p1_lx(int m32, int lane) {
  const int r = lane & 31;
  return (16 + 32 * m32 + r) * 128 + ((((lane >> 5)) ^ ((r >> 1) & 7)) * 16);
}
DI unsigned p1_lw(int T, int lane) {
  const int h = lane >> 5, q = (lane >> 2) & 3, p = lane & 3, cb = (lane >> 4) & 1;
  return (8 * h + q) * 512 + ((64 * T + 32 * cb + 8 * p) ^ ((q << 6) | (h << 5)));
}

template <int NMM>
DI void p1_mlp(unsigned smb, int w, int lane, int f0, int rt, const u16* __restrict__ b1, rsrc_t ws TSARG) {
  const int h = lane >> 5;
  const int nh = w >> 1;
  const int ma = 2 * (w & 1) + nh, mb = ma ^ 1;
  const int T[4] = {3 * nh, 3 * nh + 1, 3 * nh + 2, 6};
  f32x16 acc[2][4]; f32x4 ah[3];
  unsigned lw[4];
  unsigned bv[4];
#pragma unroll
  for (int n = 0; n < 4; ++n) {
    lw[n] = p1_lw(T[n], lane);
    if (n < 3 || NMM > 6)
      asm volatile("global_load_ushort %0, %1, %2" : "=v"(bv[n]) : "v"((unsigned)((f0 + 32 * T[n] + (lane & 31)) * 2)), "s"(b1) : "memory");
    acc[0][n] = f32x16{}; acc[1][n] = f32x16{};
    asm volatile("" : "+v"(acc[0][n]), "+v"(acc[1][n]));
  }
  const unsigned lh[3] = {0u, 0u, 0u};
  p1_compute<NMM, 0>(acc, ah, smb, p1_lx(ma, lane), p1_lx(mb, lane), lw, 0u, lh TSPASS);
  {
    WAIT_VM(0);
    const u32x4 one = {h ? 0u : 0x3f80u, 0u, 0u, 0u};
#pragma unroll
    for (int mm = 0; mm < NMM; ++mm) {
      const int n = mm >> 1, m = mm & 1;
      asm volatile("" : "+v"(bv[n]));
      const u32x4 bf = {h ? 0u : bv[n], 0u, 0u, 0u};
      acc[m][n] = MFMA32(__builtin_bit_cast(bf16x8, bf), __builtin_bit_cast(bf16x8, one), acc[m][n]);
    }
  }
  asm volatile("s_nop 15\n\ts_nop 15" : "+v"(acc[0][0]), "+v"(acc[1][0]), "+v"(acc[0][1]), "+v"(acc[1][1]), "+v"(acc[0][2]), "+v"(acc[1][2]));
  if (NMM > 6) asm volatile("" : "+v"(acc[0][3]));
#pragma unroll
  for (int mm = 0; mm < NMM; ++mm) {
    const int n = mm >> 1, m = mm & 1;
    const unsigned dst = ((40 + (f0 >> 4) + 2 * T[n]) * 64 + rt * 4 + (m ? mb : ma)) * 1024 + lane * 16;
#pragma unroll
    for (int u = 0; u < 2; ++u) {
      const u32x4 o = mish8(acc[m][n], 8 * u);
      st128_sc1(ws, dst + u * 65536, o);
    }
  }
}

constexpr int XMT = 7680, XDUMP = 9 * XMT;
DI unsigned xslot_off(int sg, int odd) {
  int part, beta; qkv_slot(sg, odd, part, beta);
  return part * 2560 + beta * 512;
}

template <int NH>
DI void p1_qkv(unsigned smb, int w, int lane, int odd TSARG) {
  const int h = lane >> 5, g = lane >> 4, i = lane & 15;
  const int ma = 2 * (w & 1), T0 = 4 * (w >> 1);
  f32x16 acc[2][4] = {}; f32x4 ah[3] = {};
  const unsigned lxh = i * 128 + ((g ^ (i >> 1)) * 16);
  const int q = (lane >> 2) & 3, p = lane & 3;
  const unsigned hrow = (8 * g + q) * 512, hsw = (q << 6) | ((g & 1) << 5);
  const int hs[3] = {2 * w, 2 * w + 1, 12 + (w & 1)};
  unsigned lh[3], lw[4];
#pragma unroll
  for (int j = 0; j < 3; ++j) lh[j] = hrow + ((32 * hs[j] + 8 * p) ^ hsw);
#pragma unroll
  for (int n = 0; n < 4; ++n) { lw[n] = p1_lw(T0 + n, lane); asm volatile("" : "+v"(acc[0][n]), "+v"(acc[1][n])); }
  p1_compute<8, NH>(acc, ah, smb, p1_lx(ma, lane), p1_lx(ma + 1, lane), lw, lxh, lh TSPASS);
  const unsigned lb = smb + (1 + 2 * ma + (g & 1)) * XMT + h * 128 + i * 8;
#pragma unroll
  for (int n = 0; n < 4; ++n)
#pragma unroll
    for (int a2 = 0; a2 < 2; ++a2) {
      const int sg = 2 * (T0 + n) + a2;
      if (n == 3 && a2 == 1 && sg == 15) continue;
      const unsigned xo = lb + xslot_off(sg, odd);
#pragma unroll
      for (int a1 = 0; a1 < 2; ++a1)
#pragma unroll
        for (int m = 0; m < 2; ++m) {
          const int a = 2 * a2 + a1;
          lds_w64(xo + a1 * 256 + m * 2 * XMT, u32x2{pack_bf16(acc[m][n][4 * a], acc[m][n][4 * a + 1]), pack_bf16(acc[m][n][4 * a + 2], acc[m][n][4 * a + 3])});
        }
    }
#pragma unroll
  for (int j = 0; j < NH; ++j)
    lds_w64(smb + xslot_off(hs[j], odd) + lane * 8, u32x2{pack_bf16(ah[j][0], ah[j][1]), pack_bf16(ah[j][2], ah[j][3])});
}

DI void p1_attn(unsigned smb, int w, int lane, int rt, int hg, rsrc_t ws) {
  const int g = lane >> 4, i = lane & 15;
  const unsigned base = smb + w * XMT;
  const unsigned pa = base + lane * 8, pe = base + (5 * g + 4) * 128 + i * 8;
  const unsigned va = base + 2560 + (lane & 3) * 128 + (lane >> 2) * 8, ve = base + 2560 + (5 * (lane & 3) + 4) * 128 + (lane >> 2) * 8;
  u32x2 q[5], kp[5], kc[5], vp[5], vc[5];
#pragma unroll
  for (int t = 0; t < 5; ++t) {
    const unsigned a = t < 4 ? pa + t * 640 : pe;
    q[t] = lds_b64(a + XMT + 5120);
    kp[t] = lds_b64(a); kc[t] = lds_b64(a + XMT);
  }
#pragma unroll
  for (int t = 0; t < 5; ++t) {
    const unsigned a = t < 4 ? va + t * 640 : ve;
    vp[t] = __builtin_bit_cast(u32x2, lds_tr(a)); vc[t] = __builtin_bit_cast(u32x2, lds_tr(a + XMT));
  }
  const int par = i & 1;
  const bool c0 = (4 * g + par) <= i, c1 = (4 * g + par + 2) <= i;
  const bool first = (rt == 0) && (w == 0);
  const unsigned hm = par ? 0xffff0000u : 0x0000ffffu;
  const unsigned m_p0 = c0 ? 0u : hm, m_c0 = c0 ? hm : 0u, m_p1 = c1 ? 0u : hm, m_c1 = c1 ? hm : 0u;
  const float cs = 0.22360679774997896f * 1.44269504088896f;
  const f32x4 z4 = {0.f, 0.f, 0.f, 0.f};
  f32x4 oe = z4;
  float inv_e = 0.f;
  u32x2 op[4];
#pragma unroll
  for (int j = 0; j < 4; ++j) {
    u32x2 qe = q[4]; if (g != j) qe = u32x2{0u, 0u};
    bf16x8 qop = mk8(q[j], qe);
    f32x4 sp = MFMA16(mk8(kp[j], kp[4]), qop, z4);
    f32x4 sc = MFMA16(mk8(kc[j], kc[4]), qop, z4);
    float a0 = par ? sc[1] : sc[0], a1 = par ? sc[3] : sc[2];
    float b0 = par ? sp[1] : sp[0], b1 = par ? sp[3] : sp[2];
    if (first) { b0 = -INFINITY; b1 = -INFINITY; }
    float s0 = c0 ? a0 : b0, s1 = c1 ? a1 : b1;
    float mx = xg_max(fmaxf(s0, s1));
    float nm = -mx * cs;
    float p0 = __builtin_amdgcn_exp2f(fmaf(s0, cs, nm)), p1 = __builtin_amdgcn_exp2f(fmaf(s1, cs, nm));
    unsigned w0 = pack_bf16(p0, p0), w1 = pack_bf16(p1, p1);
    float sum = xg_sum(bf_lo(w0) + bf_lo(w1));
    float inv = __builtin_amdgcn_rcpf(sum);
    u32x4 pw = {w0 & m_p0, w1 & m_p1, w0 & m_c0, w1 & m_c1};
    bf16x8 pop = __builtin_bit_cast(bf16x8, pw);
    f32x4 o = MFMA16(mk8(vp[j], vc[j]), pop, z4);
    u32x2 ep = vp[4], ec = vc[4];
    if ((i >> 2) != j) { ep = u32x2{0u, 0u}; ec = u32x2{0u, 0u}; }
    oe = MFMA16(mk8(ep, ec), pop, oe);
    if (g == j) inv_e = inv;
    op[j] = u32x2{pack_bf16(o[0] * inv, o[1] * inv), pack_bf16(o[2] * inv, o[3] * inv)};
  }
  const int m32 = rt * 4 + (w >> 1);
  const unsigned lp = (32 * (g & 1) + 16 * (w & 1) + i) * 16;
  const unsigned d0 = ((5 * hg + (g >> 1)) * 64 + m32) * 1024 + lp;
  st128_sc1(ws, d0, u32x4{op[0][0], op[0][1], op[1][0], op[1][1]});
  st128_sc1(ws, d0 + 2 * 65536, u32x4{op[2][0], op[2][1], op[3][0], op[3][1]});
  st64_sc1(ws, ((5 * hg + 4) * 64 + m32) * 1024 + lp + (g >> 1) * 8,
           u32x2{pack_bf16(oe[0] * inv_e, oe[1] * inv_e), pack_bf16(oe[2] * inv_e, oe[3] * inv_e)});
}

template <int NM> struct Frag2 { bf16x8 a[2][NM]; s16x4 lo[2], hi[2]; };
template <int NM>
DI void p2_stage(const Frag2<NM>& C, Frag2<NM>& N, unsigned pa, unsigned pw, f32x16 (&acc)[NM]) {
#pragma unroll
  for (int k = 0; k < 2; ++k) {
#pragma unroll
    for (int m = 0; m < NM; ++m) {
      acc[m] = MFMA32(cat(C.lo[k], C.hi[k]), C.a[k][m], acc[m]);
      if (m == 0) { N.lo[k] = lds_tr(pw + k * 2048); N.hi[k] = lds_tr(pw + k * 2048 + 1024); }
      N.a[k][m] = lds_b128(pa + (k * NM + m) * 1024);
      SCHED();
    }
  }
}
constexpr int RED_OFF = 16384;
template <int NM>
DI void p2_compute(unsigned smb, int w, int lane, int m0, GAS float* gate TSARG) {
  const int kh = w >> 1, nh = w & 1, h = lane >> 5;
  constexpr int D = Ring<NM>::D, A_SLOT = Ring<NM>::A_SLOT;
  const unsigned la = smb + kh * (2 * NM * 1024) + lane * 16;
  const int q = (lane >> 2) & 3;
  const unsigned lw = smb + Ring<NM>::W_RING + kh * 4096 + (4 * h + q) * 128 + ((64 * nh + 32 * ((lane >> 4) & 1) + 8 * (lane & 3)) ^ ((q >> 1) << 6));
  f32x16 accA[NM] = {}, accB[NM] = {};
  Frag2<NM> F0, F1;
  WAIT_VM(0);
  BARRIER_RAW();
  ST(7);
  float gv[NM];
#pragma unroll
  for (int m = 0; m < NM; ++m) gv[m] = __hip_atomic_load(gate + (m0 + m) * 32 + (lane & 31), RLX_AGENT);
#pragma unroll
  for (int k = 0; k < 2; ++k) {
    F0.lo[k] = lds_tr(lw + k * 2048); F0.hi[k] = lds_tr(lw + k * 2048 + 1024);
#pragma unroll
    for (int m = 0; m < NM; ++m) F0.a[k][m] = lds_b128(la + (k * NM + m) * 1024);
  }
  unsigned ca = A_SLOT, cw = W_SLOT;
  auto adv = [&]() { ca = (ca == (D - 1) * A_SLOT) ? 0 : ca + A_SLOT; cw = (cw == (D - 1) * W_SLOT) ? 0 : cw + W_SLOT; };
#pragma unroll 1
  for (int t = 0; t < 26; t += 2) {
    BARRIER_RAW(); p2_stage<NM>(F0, F1, la + ca, lw + cw, accB); adv();
    BARRIER_RAW(); p2_stage<NM>(F1, F0, la + ca, lw + cw, accB); adv();
  }
  BARRIER_RAW(); p2_stage<NM>(F0, F1, la + ca, lw + cw, accB); adv();
  ST(8);
  BARRIER_RAW(); p2_stage<NM>(F1, F0, la + ca, lw + cw, accA); adv();
#pragma unroll 1
  for (int t = 28; t < NST - 1; t += 2) {
    BARRIER_RAW(); p2_stage<NM>(F0, F1, la + ca, lw + cw, accA); adv();
    BARRIER_RAW(); p2_stage<NM>(F1, F0, la + ca, lw + cw, accA); adv();
  }
#pragma unroll
  for (int k = 0; k < 2; ++k)
#pragma unroll
    for (int m = 0; m < NM; ++m) accA[m] = MFMA32(cat(F0.lo[k], F0.hi[k]), F0.a[k][m], accA[m]);
  ST(9);
  BARRIER();
  f32x4 keep[NM][2];
#pragma unroll
  for (int m = 0; m < NM; ++m) {
    const float hv = 1.f - gv[m];
#pragma unroll
    for (int a = 0; a < 2; ++a) {
      f32x4 lo, hi;
#pragma unroll
      for (int e = 0; e < 4; ++e) {
        lo[e] = gv[m] * accA[m][4 * a + e] + hv * accB[m][4 * a + e];
        hi[e] = gv[m] * accA[m][8 + 4 * a + e] + hv * accB[m][8 + 4 * a + e];
      }
      keep[m][a] = kh ? hi : lo;
      *(LAS f32x4*)(size_t)(smb + RED_OFF + ((w * NM + m) * 2 + a) * 1024 + lane * 16) = kh ? lo : hi;
    }
  }
  BARRIER();
#pragma unroll
  for (int m = 0; m < NM; ++m)
#pragma unroll
    for (int a = 0; a < 2; ++a) {
      const f32x4 o = keep[m][a] + *(LAS f32x4*)(size_t)(smb + RED_OFF + (((w ^ 2) * NM + m) * 2 + a) * 1024 + lane * 16);
      lds_w64(smb + (m * 32 + (lane & 31)) * 144 + nh * 64 + (2 * kh + a) * 16 + h * 8, u32x2{pack_bf16(o[0], o[1]), pack_bf16(o[2], o[3])});
    }
}

__global__ __launch_bounds__(NTHREADS, 2) void fused_kernel(const u16* __restrict__ x, const u16* __restrict__ wqkv, const u16* __restrict__ wo,
                                                            const u16* __restrict__ w1, const u16* __restrict__ b1, const u16* __restrict__ w2,
                                                            const u16* __restrict__ gw, char* __restrict__ wsp, u16* __restrict__ out) {
  extern __shared__ __attribute__((aligned(16))) char smem[];
  const unsigned smb = (unsigned)(size_t)(LAS char*)smem;
  TSDECL; ST(0);
  const int lane = threadIdx.x & 63;
  const int w = __builtin_amdgcn_readfirstlane(threadIdx.x >> 6);
  int b = blockIdx.x;
  const int is_mlp = (b >> 3) & 1;
  const int rt = ((b >> 1) & 3) * 4 + ((b >> 4) & 3);
  const int cg = (b & 1) * 4 + (b >> 6);
  const int row0 = rt * 128;
  const int ps = cg < 7 ? 7 * cg : 48, np = cg < 6 ? 7 : 6;
  int k0 = ((5 * (rt & 3)) >> 1) + ((5 * (is_mlp * 4 + (cg & 3))) >> 2);
  k0 = ROT1 == 2 ? (k0 >= 9 ? k0 - 8 : k0 + 1) : (ROT1 ? (k0 >= 10 ? k0 - 10 : k0) : 0);
  const char* wmat = is_mlp ? (const char*)w1 : (const char*)wqkv;
  const unsigned vwme = p1_vw(w, lane, is_mlp, cg, ps, np);
  {
    const int kf = ROT1 == 2 ? 0 : k0;
    const unsigned RB = is_mlp ? 3456u : 3840u;
    const char* xs = (const char*)x + kf * 128;
    const char* wsrc = wmat + kf * (64 * RB);
    const unsigned d = smb + w * 1024;
    const unsigned vx = (row0 + 8 * w + (lane >> 3)) * 1280 + (((lane & 7) ^ (4 * (w & 1) + (lane >> 4))) * 16);
    glds16(xs, vx, d + 2048);
    glds16(wsrc, vwme, d + W_OFF);
    glds16(xs + 64 * 1280, vx, d + 2048 + 8192);
    glds16(wsrc + 16 * RB, vwme, d + W_OFF + 8192);
    glds16(wsrc + 32 * RB, vwme, d + W_OFF + 16384);
    glds16(wsrc + 48 * RB, vwme, d + W_OFF + 24576);
    int tokh = row0 - 16 + 2 * w + (lane >> 5); tokh = tokh < 0 ? 0 : tokh;
    const unsigned vh = tokh * 1280 + (((((lane & 31) >> 2) ^ w) * 16) | ((lane & 3) * 4));
    asm volatile("s_mov_b32 m0, %2\n\ts_nop 0\n\tglobal_load_lds_dword %0, %1" : : "v"(vh), "s"(xs), "s"(smb + w * 256) : "memory");
  }
  if (ROT1 != 2) k0 = p1_next(k0);
  if (w >= 4) {
    {
      const char* pc;
      asm volatile("s_getpc_b64 %0" : "=s"(pc));
      const int chunk = ((b >> 3) * 4 + w) % CODE_CHUNKS;
      asm volatile("s_mov_b32 m0, %2\n\ts_mov_b32 exec_hi, 0\n\tglobal_load_lds_dword %0, %1\n\ts_mov_b32 exec_hi, -1"
                   : : "v"((unsigned)(chunk * 4096 + lane * 128)), "s"(pc), "s"(smb + 3 * K1_STAGE + w * 256) : "memory");
    }
    const unsigned vw0 = p1_vw(w - 4, lane, is_mlp, cg, ps, np);
    if (is_mlp) p1_loader<0>(smb, w - 4, lane, (const char*)x, wmat, row0, vw0, vwme, k0 TSPASS);
    else p1_loader<1>(smb, w - 4, lane, (const char*)x, wmat, row0, vw0, vwme, k0 TSPASS);
  }
  asm volatile("" : "+s"(b));
  const int xcd = b & 7, idx = b >> 3;
  int ct, m0;
  {
    const int j = idx >> 1, jj = j - 6;
    const int ctm = j < 12 ? j >> 2 : 3 + ((j - 12) >> 1), m0m = 3 * (j < 12 ? j & 3 : j & 1);
    const int ctq = j < 6 ? j >> 1 : 3 + (jj >= 5), m0q = j < 6 ? 12 + 2 * (j & 1) : 6 + 2 * (jj >= 5 ? jj - 5 : jj);
    ct = is_mlp ? ctm : ctq; m0 = is_mlp ? m0m : m0q;
  }
  int s0 = ROT2 ? ((m0 * 27) >> 4) + ct * 5 : 0;
  s0 = s0 >= 27 ? s0 - 27 : s0;
  ct += (xcd & 1) * 5; m0 += (xcd >> 1) * 16;
  const int nm = is_mlp ? 3 : 2;
  const rsrc_t ws = __builtin_amdgcn_make_buffer_rsrc(wsp, 0, WS_BYTES, 0x00020000);
  GAS u64* flags = (GAS u64*)(wsp + FLAG_OFF);
  GAS u64* epoch = (GAS u64*)(wsp + EPOCH_OFF);
  GAS float* gate = (GAS float*)(wsp + GATE_OFF);
  WLoad wl = {0u, 0u, nullptr, 0};
  u64 tag = 0;
  if (w >= 4) {
    wl = is_mlp ? p2_w_prologue<3>(smb, w - 4, lane, ct, s0, w2) : p2_w_prologue<2>(smb, w - 4, lane, ct, s0, w2);
    if (w < 6) {
      const int id = idx * 100 + (w - 4) * 50 + (lane < 50 ? lane : 49);
      const int row = id / 5;
      unsigned dm;
      asm volatile("global_load_dword %0, %1, %2" : "=v"(dm) : "v"((unsigned)(row * 1280 + ((xcd & 1) * 5 + (id - row * 5)) * 128)), "s"(wo) : "memory");
      BARRIER_RAW();
      asm volatile("s_waitcnt vmcnt(0)" : "+v"(dm) :: "memory");
    } else
    BARRIER_RAW();
    tag = __hip_atomic_load(epoch, RLX_AGENT) * 0x9E3779B97F4A7C15ull + 0x632BE59BD9B4E019ull;
    if (w >= 6) {
      if (is_mlp) {
        const int row = row0 + cg * 16 + (w - 6) * 8 + (lane >> 3);
        const char* xr = (const char*)x + row * 1280 + (lane & 7) * 16;
        const char* gr = (const char*)gw + (lane & 7) * 16;
        float s = 0.f;
#pragma unroll
        for (int t = 0; t < 10; ++t) {
          u32x4 a = *(const u32x4*)(xr + t * 128), c = *(const u32x4*)(gr + t * 128);
#pragma unroll
          for (int e = 0; e < 4; ++e) {
            const unsigned ua = a[e], uc = c[e];
            s = __builtin_amdgcn_fdot2_f32_bf16(__builtin_bit_cast(bf16x2_t, ua), __builtin_bit_cast(bf16x2_t, uc), s, false);
          }
        }
        s += __shfl_xor(s, 1); s += __shfl_xor(s, 2); s += __shfl_xor(s, 4);
        if ((lane & 7) == 0) __hip_atomic_store(gate + row, 1.f / (1.f + __expf(-s)), RLX_AGENT);
      }
    }
    ST(1); ST(2);
  } else if (is_mlp) {
    const int f0 = ps * 32;
    if (np == 7) p1_mlp<7>(smb, w, lane, f0, rt, b1, ws TSPASS);
    else p1_mlp<6>(smb, w, lane, f0, rt, b1, ws TSPASS);
  } else {
    if (w < 2) p1_qkv<3>(smb, w, lane, cg & 1 TSPASS);
    else p1_qkv<2>(smb, w, lane, cg & 1 TSPASS);
  }
  if (!is_mlp) {
    BARRIER();
    ST(3);
    p1_attn(smb, w, lane, rt, cg, ws);
  } else { ST(3); }
  ST(4);
  GAS u64* myflag = flags + rt * 16 + is_mlp * 8 + cg;
  if (is_mlp) WAIT_VM(0);
  BARRIER();
  ST(5);
  if (is_mlp && w == 6 && lane == 0) __hip_atomic_store(myflag, tag, RLX_AGENT);
  ST(6);
  if (w >= 4) {
    const int rta = m0 >> 2, rtb = (m0 + nm - 1) >> 2;
    GAS u64* fq = flags + ((lane & 8) ? rtb : rta) * 16 + (lane & 7);
    if (is_mlp) p2_loader<3>(smb, w - 4, lane, m0, s0, wl, wsp, wo, fq + 8, fq, tag, myflag);
    else p2_loader<2>(smb, w - 4, lane, m0, s0, wl, wsp, wo, fq + 8, fq, tag, myflag);
    ST(7); ST(8); ST(9);
    BARRIER_RAW();
    BARRIER_RAW();
  } else if (is_mlp) p2_compute<3>(smb, w, lane, m0, gate TSPASS);
  else p2_compute<2>(smb, w, lane, m0, gate TSPASS);
  BARRIER();
  for (int c = threadIdx.x; c < nm * 256; c += NTHREADS) {
    const int r = c >> 3, ch = c & 7;
    u32x4 v = *(LAS u32x4*)(size_t)(smb + r * 144 + ch * 16);
    *(u32x4*)((char*)out + (size_t)(m0 * 32 + r) * 1280 + ct * 128 + ch * 16) = v;
  }
  if (b == 254 && w == 7) {
    for (int spin = 0; spin < SPIN_CAP; ++spin) {
      bool ok = true;
#pragma unroll
      for (int k = 0; k < 4; ++k) ok &= __hip_atomic_load(flags + k * 64 + lane, RLX_AGENT) == tag;
      if (__all(ok)) break;
      __builtin_amdgcn_s_sleep(8);
    }
    if (lane == 0) __hip_atomic_store(epoch, tag, RLX_AGENT);
  }
  ST(10);
  if (w == 0 && is_mlp == PROBECLS) { unsigned long long e0 = __builtin_amdgcn_s_memrealtime(); while (__builtin_amdgcn_s_memrealtime() - e0 < ts.tp - ts.t0) __builtin_amdgcn_s_sleep(1); }
}

extern "C" void kernel_launch(void* const* d_in, const int* in_sizes, int n_in, void* d_out, int out_size, void* d_ws,
                              size_t ws_size, hipStream_t stream) {
  (void)hipFuncSetAttribute((const void*)fused_kernel, hipFuncAttributeMaxDynamicSharedMemorySize, LDS_BYTES);
  fused_kernel<<<256, NTHREADS, LDS_BYTES, stream>>>((const u16*)d_in[0], (const u16*)d_in[1], (const u16*)d_in[2], (const u16*)d_in[4],
                                                     (const u16*)d_in[5], (const u16*)d_in[6], (const u16*)d_in[7], (char*)d_ws, (u16*)d_out);
}
```

```cpp
#include <hip/hip_runtime.h>
#include <stdint.h>
#include <type_traits>

typedef unsigned short u16;
typedef unsigned long long u64;
typedef short bf16x8 __attribute__((ext_vector_type(8)));
typedef short s16x4 __attribute__((ext_vector_type(4)));
typedef float f32x4 __attribute__((ext_vector_type(4)));
typedef float f32x16 __attribute__((ext_vector_type(16)));
typedef float f32x2 __attribute__((ext_vector_type(2)));
typedef unsigned u32x4 __attribute__((ext_vector_type(4)));
typedef unsigned u32x2 __attribute__((ext_vector_type(2)));
typedef unsigned u32x16 __attribute__((ext_vector_type(16)));
typedef __bf16 bf16x2_t __attribute__((ext_vector_type(2)));
#define LAS __attribute__((address_space(3)))
#define GAS __attribute__((address_space(1)))
#define DI __device__ __forceinline__

#define MFMA16(a, b, c) __builtin_amdgcn_mfma_f32_16x16x32_bf16((a), (b), (c), 0, 0, 0)
#define MFMA32(a, b, c) __builtin_amdgcn_mfma_f32_32x32x16_bf16((a), (b), (c), 0, 0, 0)
#define WAIT_VM(n) asm volatile("s_waitcnt vmcnt(" #n ")" ::: "memory")
#define WAIT_VMI(n) asm volatile("s_waitcnt vmcnt(%0)" ::"i"(n) : "memory")
#define BARRIER() do { asm volatile("s_waitcnt lgkmcnt(0)" ::: "memory"); __builtin_amdgcn_s_barrier(); asm volatile("" ::: "memory"); } while (0)
#define BARRIER_RAW() do { asm volatile("" ::: "memory"); __builtin_amdgcn_s_barrier(); asm volatile("" ::: "memory"); } while (0)
#define SCHED() __builtin_amdgcn_sched_barrier(0)
#define RLX_AGENT __ATOMIC_RELAXED, __HIP_MEMORY_SCOPE_AGENT

#define TSARG
#define TSPASS
#define TSDECL
#define ST(id)
#define TSOUT()

constexpr int NTHREADS = 512;
constexpr int NKAP = 148;
constexpr int W_OFF = 18432;
constexpr int K1_STAGE = 51200;
constexpr unsigned MID_BYTES = NKAP * 64 * 1024;
constexpr unsigned GATE_OFF = MID_BYTES, FLAG_OFF = GATE_OFF + 8192, EPOCH_OFF = FLAG_OFF + 2048, WS_BYTES = EPOCH_OFF + 128;
constexpr int LDS_BYTES = 163840;
constexpr int W_SLOT = 8192;
template <int NM> struct Ring {
  static constexpr int D = NM == 3 ? 8 : 10;
  static constexpr int A_SLOT = NM * 4096;
  static constexpr int W_RING = D * A_SLOT;
};
constexpr int NST = NKAP / 4;
constexpr int SPIN_CAP = 1 << 22;
constexpr int PFN = 1;
constexpr int CODE_CHUNKS = 11;

DI unsigned pack_bf16(float lo, float hi) {
  f32x2 f = {lo, hi};
  return __builtin_bit_cast(unsigned, __builtin_convertvector(f, bf16x2_t));
}
DI float bf_lo(unsigned u) { return __uint_as_float(u << 16); }
DI float bf_hi(unsigned u) { return __uint_as_float(u & 0xffff0000u); }
DI void glds16(const char* sbase, unsigned voff, unsigned dst) {
  asm volatile("s_mov_b32 m0, %2\n\ts_nop 0\n\tglobal_load_lds_dwordx4 %0, %1" : : "v"(voff), "s"(sbase), "s"(dst) : "memory");
}
DI void glds16_sc1(const char* sbase, unsigned voff, unsigned dst) {
  asm volatile("s_mov_b32 m0, %2\n\ts_nop 0\n\tglobal_load_lds_dwordx4 %0, %1 sc1" : : "v"(voff), "s"(sbase), "s"(dst) : "memory");
}
DI void glds16_lo(const char* sbase, unsigned voff, unsigned dst) {
  asm volatile("s_mov_b32 m0, %2\n\ts_mov_b32 exec_hi, 0\n\tglobal_load_lds_dwordx4 %0, %1\n\ts_mov_b32 exec_hi, -1" : : "v"(voff), "s"(sbase), "s"(dst) : "memory");
}
DI bf16x8 lds_b128(unsigned a) { return *(LAS bf16x8*)(size_t)a; }
DI s16x4 lds_tr(unsigned a) { return __builtin_amdgcn_ds_read_tr16_b64_v4i16((LAS s16x4*)(size_t)a); }
DI u32x2 lds_b64(unsigned a) { return *(LAS u32x2*)(size_t)a; }
DI void lds_w64(unsigned a, u32x2 v) { *(LAS u32x2*)(size_t)a = v; }
DI bf16x8 cat(s16x4 lo, s16x4 hi) { return __builtin_shufflevector(lo, hi, 0, 1, 2, 3, 4, 5, 6, 7); }
DI bf16x8 mk8(u32x2 a, u32x2 b) {
  u32x4 t = {a[0], a[1], b[0], b[1]};
  return __builtin_bit_cast(bf16x8, t);
}
typedef __amdgpu_buffer_rsrc_t rsrc_t;
#define STAUX 16
DI void st128_sc1(rsrc_t r, unsigned off, u32x4 v) { __builtin_amdgcn_raw_buffer_store_b128(v, r, off, 0, STAUX); }
DI void st64_sc1(rsrc_t r, unsigned off, u32x2 v) { __builtin_amdgcn_raw_buffer_store_b64(v, r, off, 0, 16); }

DI u32x4 mish8(const f32x16& v, int o) {
  f32x2 h[4], z[4], e[4], p[4], d[4], r[4];
  const f32x2 c1 = {1.44269504088896f / 160.f, 1.44269504088896f / 160.f}, c0 = {129.f / 160.f, 129.f / 160.f};
#pragma unroll
  for (int k = 0; k < 4; ++k) { h[k] = f32x2{v[o + 2 * k], v[o + 2 * k + 1]}; asm("v_pk_fma_f32 %0, %1, %2, %3 clamp" : "=v"(z[k]) : "v"(h[k]), "v"(c1), "v"(c0)); }
#pragma unroll
  for (int k = 0; k < 4; ++k) z[k] = z[k] * 160.f - 129.f;
  SCHED();
#pragma unroll
  for (int k = 0; k < 4; ++k) e[k] = f32x2{__builtin_amdgcn_exp2f(z[k][0]), __builtin_amdgcn_exp2f(z[k][1])};
  SCHED();
#pragma unroll
  for (int k = 0; k < 4; ++k) p[k] = e[k] + 2.f;
#pragma unroll
  for (int k = 0; k < 4; ++k) d[k] = e[k] * p[k] + 2.f;
  SCHED();
#pragma unroll
  for (int k = 0; k < 4; ++k) r[k] = f32x2{__builtin_amdgcn_rcpf(d[k][0]), __builtin_amdgcn_rcpf(d[k][1])};
  SCHED();
#pragma unroll
  for (int k = 0; k < 4; ++k) h[k] = h[k] * e[k];
#pragma unroll
  for (int k = 0; k < 4; ++k) h[k] = h[k] * p[k];
#pragma unroll
  for (int k = 0; k < 4; ++k) h[k] = h[k] * r[k];
  u32x4 o4;
#pragma unroll
  for (int k = 0; k < 4; ++k) o4[k] = pack_bf16(h[k][0], h[k][1]);
  SCHED();
  return o4;
}
DI float xg_max(float v) {
  unsigned u = __float_as_uint(v);
  auto r = __builtin_amdgcn_permlane32_swap(u, u, false, false);
  v = fmaxf(__uint_as_float(r[0]), __uint_as_float(r[1]));
  u = __float_as_uint(v);
  auto r2 = __builtin_amdgcn_permlane16_swap(u, u, false, false);
  return fmaxf(__uint_as_float(r2[0]), __uint_as_float(r2[1]));
}
DI float xg_sum(float v) {
  unsigned u = __float_as_uint(v);
  auto r = __builtin_amdgcn_permlane32_swap(u, u, false, false);
  v = __uint_as_float(r[0]) + __uint_as_float(r[1]);
  u = __float_as_uint(v);
  auto r2 = __builtin_amdgcn_permlane16_swap(u, u, false, false);
  return __uint_as_float(r2[0]) + __uint_as_float(r2[1]);
}

#define RAMP0 2
template <int D>
constexpr int p2_issued(int t) {
  int n = RAMP0;
  for (int i = 0; i <= t; ++i) { n += 2; if (n > i + D - 1) n = i + D - 1; }
  return n > NST ? NST : n;
}
template <int NM, int D>
constexpr int p2_allowed(int t) {
  int n = 0;
  for (int st = t + 2; st < p2_issued<D>(t); ++st) n += st < D - 1 ? NM : NM + 2;
  return n;
}
template <int I, int N, class F>
DI void static_for(F&& f) {
  if constexpr (I < N) { f(std::integral_constant<int, I>{}); static_for<I + 1, N>(f); }
}
struct WLoad { unsigned voffn, voffe; const char* src; int st; };
template <int NM>
DI WLoad p2_w_prologue(unsigned smb, int l, int lane, int ct, int s0, const u16* __restrict__ w2) {
  const int r8 = lane >> 3;
  const unsigned colb = ct * 128 + (((lane & 7) ^ (((r8 >> 1) & 1) << 2)) * 16);
  WLoad s;
  s.voffn = r8 * 1280 + colb;
  s.voffe = (20 * (r8 >> 2) + (r8 & 3)) * 1280 + colb;
  s.src = (const char*)w2 + (16 * l) * 1280;
  const unsigned d = smb + Ring<NM>::W_RING + l * 2048;
  s.st = s0;
#pragma unroll 1
  for (int t = 0; t < Ring<NM>::D - 1; ++t) {
    const char* p = s.src + s.st * (64 * 1280);
    glds16(p, s.voffn, d + t * W_SLOT);
    glds16(p + 8 * 1280, s.voffn, d + t * W_SLOT + 1024);
    s.st = s.st == 26 ? 0 : s.st + 1;
  }
  return s;
}
DI void spin_flags(GAS u64* fp, u64 tag) {
#pragma unroll 1
  for (int spin = 0; spin < SPIN_CAP; ++spin) {
    if (__all(__hip_atomic_load(fp, RLX_AGENT) == tag)) break;
    __builtin_amdgcn_s_sleep(2);
  }
  asm volatile("" ::: "memory");
}
template <int NM>
DI void p2_loader(unsigned smb, int l, int lane, int m0, int sa, WLoad& ws, const char* __restrict__ mid, const u16* __restrict__ wo,
                  GAS u64* fpm, GAS u64* fpq, u64 tag, GAS u64* myflag) {
  constexpr int D = Ring<NM>::D, A_SLOT = Ring<NM>::A_SLOT;
  const unsigned lane16 = lane * 16;
  const char* src = mid + (size_t)((40 + l) * 64 + m0) * 1024;
  const unsigned da = smb + l * (NM * 1024), dw = smb + Ring<NM>::W_RING + l * 2048;
  auto issue_a = [&](unsigned slot) {
    const char* p = src + (size_t)sa * (4 * 65536);
#pragma unroll
    for (int m = 0; m < NM; ++m) glds16_sc1(p + m * 1024, lane16, da + slot * A_SLOT + m * 1024);
    sa = sa == 26 ? 0 : sa + 1;
  };
  spin_flags(fpm, tag);
#pragma unroll
  for (int t = 0; t < RAMP0; ++t) issue_a(t);
  WAIT_VMI((p2_allowed<NM, D>(-1)));
  BARRIER_RAW();
  if (NM == 2 && l == 2 && lane == 0) {
    asm volatile("global_store_dwordx2 %0, %1, off sc1" : : "v"(myflag), "v"(tag) : "memory");
  }
  unsigned ps = D - 1;
  u64 fq = 0;
  auto issue_act = [&]() {
    const char* p = ws.src + sa * (64 * 1280);
    glds16(p, ws.voffn, dw + ps * W_SLOT);
    glds16(p + 8 * 1280, ws.voffn, dw + ps * W_SLOT + 1024);
    issue_a(ps);
    ps = (ps == D - 1) ? 0 : ps + 1;
  };
  auto issue_range = [&](auto c0, auto c1) {
    static_for<c0.value, c1.value>([&](auto sc) { if constexpr (sc.value < D - 1) issue_a(sc.value); else issue_act(); });
  };
  issue_range(std::integral_constant<int, p2_issued<D>(-1)>{}, std::integral_constant<int, p2_issued<D>(0)>{});
  static_for<0, D - 3>([&](auto tc) {
    WAIT_VMI((p2_allowed<NM, D>(tc.value))); BARRIER_RAW();
    issue_range(std::integral_constant<int, p2_issued<D>(tc.value)>{}, std::integral_constant<int, p2_issued<D>(tc.value + 1)>{});
  });
  static_assert(p2_issued<D>(D - 3) == 2 * D - 4, "ring must be full when the steady state starts");
#pragma unroll 1
  for (int t = D - 3; t + D - 1 <= 24; ++t) { WAIT_VMI((NM + 2) * (D - 3)); BARRIER_RAW(); issue_act(); }
  asm volatile("global_load_dwordx2 %0, %1, off sc1" : "=v"(fq) : "v"(fpq) : "memory");
  WAIT_VMI((NM + 2) * (D - 3)); BARRIER_RAW(); issue_act();
  WAIT_VMI((NM + 2) * (D - 3)); BARRIER_RAW(); issue_act();
  asm volatile("s_waitcnt vmcnt(%1)" : "+v"(fq) : "i"(2 * (NM + 2)) : "memory");
  if (!__all(fq == tag)) spin_flags(fpq, tag);
  src = mid + (size_t)(l * 64 + m0) * 1024; sa = 0;
  int u = l;
  const char* won = (const char*)wo;
#pragma unroll 1
  for (int j = 0; j < 10; ++j) {
    WAIT_VMI((NM + 2) * (D - 3)); BARRIER_RAW();
    const int r0 = u < 4 ? 40 * (u >> 1) + 8 * (u & 1) : 16, dr = u < 4 ? 20 : 40;
    const unsigned vo = u < 4 ? ws.voffn : ws.voffe;
    glds16(won + r0 * 1280, vo, dw + ps * W_SLOT);
    glds16(won + (r0 + dr) * 1280, vo, dw + ps * W_SLOT + 1024);
    won += u >= 1 ? 80 * 1280 : 0;
    u = u >= 1 ? u - 1 : 4;
    issue_a(ps);
    ps = (ps == D - 1) ? 0 : ps + 1;
  }
  static_for<NST - D + 1, NST - 1>([&](auto tc) { WAIT_VMI((p2_allowed<NM, D>(tc.value))); BARRIER_RAW(); });
}

DI void qkv_slot(int sg, int odd, int& P, int& beta) {
  if (sg < 12) { P = sg >> 2; beta = (sg & 3) + odd; }
  else { P = sg < 15 ? sg - 12 : 2; beta = odd ? 0 : 4; }
}
DI unsigned p1_vw(int v, int lane, int is_mlp, int cg, int ps, int np) {
  const int hi = lane >> 5;
  const int c = (lane & 31) ^ ((((2 * v + hi) & 3) << 2) | ((v >> 2) << 1));
  const int sg = c >> 1, odd = cg & 1;
  const int P = sg < 12 ? sg >> 2 : (sg < 15 ? sg - 12 : 2);
  const int beta = sg < 12 ? (sg & 3) + odd : (odd ? 0 : 4);
  const unsigned colq = (P == 2 ? 0 : (P + 1) * 1280) + cg * 160 + beta * 32 + (c & 1) * 16;
  const unsigned colm = ps * 64 + (c < 4 * np ? c : 4 * np - 1) * 16;
  return (2 * v + hi) * (is_mlp ? 3456u : 3840u) + (is_mlp ? colm : colq);
}
#define ROT1 2
#define ROT2 1
#define PRE1 5
DI int p1_next(int kk) { return kk == 9 ? (ROT1 == 2 ? 1 : 0) : kk + 1; }
template <int QKV>
DI void p1_loader(unsigned smb, int L, int lane, const char* __restrict__ x, const char* __restrict__ wmat, int row0, unsigned vw0, unsigned vw1, int kk TSARG) {
  constexpr unsigned RB = QKV ? 3840 : 3456;
  constexpr int NL = QKV ? 13 : 12;
  const unsigned vx = (row0 + 8 * L + (lane >> 3)) * 1280 + (((lane & 7) ^ (4 * (L & 1) + (lane >> 4))) * 16);
  const int hr = 4 * L + ((lane >> 3) & 3);
  int tokh = row0 - 16 + hr; tokh = tokh < 0 ? 0 : tokh;
  const unsigned vh = tokh * 1280 + (((lane & 7) ^ ((hr >> 1) & 7)) * 16);
  auto piece = [&](int p, unsigned buf) {
    const unsigned d = smb + buf + L * 1024;
    const char* xs = x + kk * 128;
    const char* wsrc = wmat + kk * (64 * RB);
    if (p < 8) { if (p & 1) glds16(xs + (p >> 1) * (32 * 1280), vx, d + 2048 + (p >> 1) * 4096);
                 else glds16(wsrc + (p >> 2) * (16 * RB), (p & 2) ? vw1 : vw0, d + W_OFF + (p >> 1) * 4096); }
    else if (p < 12) glds16(wsrc + ((p - 4) >> 1) * (16 * RB), (p & 1) ? vw1 : vw0, d + W_OFF + (p - 4) * 4096);
    else glds16_lo(xs, vh, smb + buf + L * 512);
  };
  auto issue = [&](unsigned buf) {
#pragma unroll
    for (int p = 0; p < NL; ++p) piece(p, buf);
    kk = p1_next(kk);
  };
#pragma unroll
  for (int p = 0; p < PRE1; ++p) piece(p, K1_STAGE);
  WAIT_VMI(PRE1 + PFN);
  BARRIER_RAW();
#pragma unroll
  for (int p = PRE1; p < NL; ++p) piece(p, K1_STAGE);
  kk = p1_next(kk);
  issue(2 * K1_STAGE);
  unsigned nb = 0;
#pragma unroll 1
  for (int t = 0; t < 7; ++t) {
    WAIT_VMI(NL);
    BARRIER_RAW();
    issue(nb);
    nb = (nb == 2 * K1_STAGE) ? 0 : nb + K1_STAGE;
  }
  WAIT_VMI(NL);
  BARRIER_RAW();
  WAIT_VM(0);
  BARRIER_RAW();
}

struct Frag { bf16x8 x[2]; s16x4 lo[4], hi[4]; };
struct HFrag { bf16x8 x; s16x4 lo[3], hi[3]; };

template <int NMM>
DI void p1_read(Frag& F, int idx, unsigned ax0, unsigned ax1, const unsigned (&aw)[4], int s) {
  if (idx == 0) F.x[0] = lds_b128(ax0);
  else if (idx == 3) F.x[1] = lds_b128(ax1);
  else if (idx < (NMM > 6 ? 10 : 8)) {
    const int k = idx < 3 ? idx - 1 : idx - 2;
    if ((k & 1) == 0) F.lo[k >> 1] = lds_tr(aw[k >> 1] + W_OFF + s * 8192);
    else F.hi[k >> 1] = lds_tr(aw[k >> 1] + W_OFF + s * 8192 + 2048);
  }
}

template <int NMM, int NH>
DI void p1_compute(f32x16 (&acc)[2][4], f32x4 (&ah)[3], unsigned smb, unsigned lx0, unsigned lx1, const unsigned (&lw)[4],
                   unsigned lxh, const unsigned (&lh)[3] TSARG) {
  Frag F[2];
  HFrag H;
  unsigned aw[4];
#pragma unroll
  for (int n = 0; n < 4; ++n) aw[n] = smb + lw[n];
  WAIT_VMI(NH ? 0 : (NMM > 6 ? 4 : 3));
  BARRIER_RAW();
  ST(1);
#pragma unroll
  for (int i = 0; i < 10; ++i) p1_read<NMM>(F[0], i, smb + lx0, smb + lx1, aw, 0);
  unsigned cur = smb;
#pragma unroll 1
  for (int t = 0; t < 10; ++t) {
    const unsigned nxt = (cur == smb + 2 * K1_STAGE) ? smb : cur + K1_STAGE;
#pragma unroll
    for (int s = 0; s < 4; ++s) {
      const Frag& C = F[s & 1];
      Frag& N = F[(s + 1) & 1];
      const unsigned nb = s == 3 ? nxt : cur;
      const int ns = (s + 1) & 3;
      const unsigned ax0 = nb + (lx0 ^ (32 * ns)), ax1 = nb + (lx1 ^ (32 * ns));
#pragma unroll
      for (int mm = 0; mm < NMM; ++mm) {
        const int n = mm >> 1, m = mm & 1;
        acc[m][n] = MFMA32(cat(C.lo[n], C.hi[n]), C.x[m], acc[m][n]);
        if (s == 3 && mm == 0) {
          SCHED();
          BARRIER();
#pragma unroll
          for (int q = 0; q < 4; ++q) aw[q] = nxt + lw[q];
        }
        p1_read<NMM>(N, 2 * mm, ax0, ax1, aw, ns); p1_read<NMM>(N, 2 * mm + 1, ax0, ax1, aw, ns);
        if (NH) {
          if (mm == 4 && (s == 1 || s == 2)) {
#pragma unroll
            for (int j = 0; j < NH; ++j) ah[j] = MFMA16(cat(H.lo[j], H.hi[j]), H.x, ah[j]);
          }
          if (mm >= 5 && s < 2) {
            if (mm == 5) H.x = lds_b128(cur + (lxh ^ (64 * s)));
            if (mm - 5 < NH) { H.lo[mm - 5] = lds_tr(cur + lh[mm - 5] + W_OFF + s * 16384); H.hi[mm - 5] = lds_tr(cur + lh[mm - 5] + W_OFF + s * 16384 + 2048); }
          }
        }
        SCHED();
      }
    }
    cur = nxt;
  }
  ST(2);
}

DI unsigned p1_lx(int m32, int lane) {
  const int r = lane & 31;
  return (16 + 32 * m32 + r) * 128 + ((((lane >> 5)) ^ ((r >> 1) & 7)) * 16);
}
DI unsigned p1_lw(int T, int lane) {
  const int h = lane >> 5, q = (lane >> 2) & 3, p = lane & 3, cb = (lane >> 4) & 1;
  return (8 * h + q) * 512 + ((64 * T + 32 * cb + 8 * p) ^ ((q << 6) | (h << 5)));
}

template <int NMM>
DI void p1_mlp(unsigned smb, int w, int lane, int f0, int rt, const u16* __restrict__ b1, rsrc_t ws TSARG) {
  const int h = lane >> 5;
  const int nh = w >> 1;
  const int ma = 2 * (w & 1) + nh, mb = ma ^ 1;
  const int T[4] = {3 * nh, 3 * nh + 1, 3 * nh + 2, 6};
  f32x16 acc[2][4]; f32x4 ah[3];
  unsigned lw[4];
  unsigned bv[4];
#pragma unroll
  for (int n = 0; n < 4; ++n) {
    lw[n] = p1_lw(T[n], lane);
    if (n < 3 || NMM > 6)
      asm volatile("global_load_ushort %0, %1, %2" : "=v"(bv[n]) : "v"((unsigned)((f0 + 32 * T[n] + (lane & 31)) * 2)), "s"(b1) : "memory");
    acc[0][n] = f32x16{}; acc[1][n] = f32x16{};
    asm volatile("" : "+v"(acc[0][n]), "+v"(acc[1][n]));
  }
  const unsigned lh[3] = {0u, 0u, 0u};
  p1_compute<NMM, 0>(acc, ah, smb, p1_lx(ma, lane), p1_lx(mb, lane), lw, 0u, lh TSPASS);
  {
    WAIT_VM(0);
    const u32x4 one = {h ? 0u : 0x3f80u, 0u, 0u, 0u};
#pragma unroll
    for (int mm = 0; mm < NMM; ++mm) {
      const int n = mm >> 1, m = mm & 1;
      asm volatile("" : "+v"(bv[n]));
      const u32x4 bf = {h ? 0u : bv[n], 0u, 0u, 0u};
      acc[m][n] = MFMA32(__builtin_bit_cast(bf16x8, bf), __builtin_bit_cast(bf16x8, one), acc[m][n]);
    }
  }
  asm volatile("s_nop 15\n\ts_nop 15" : "+v"(acc[0][0]), "+v"(acc[1][0]), "+v"(acc[0][1]), "+v"(acc[1][1]), "+v"(acc[0][2]), "+v"(acc[1][2]));
  if (NMM > 6) asm volatile("" : "+v"(acc[0][3]));
#pragma unroll
  for (int mm = 0; mm < NMM; ++mm) {
    const int n = mm >> 1, m = mm & 1;
    const unsigned dst = ((40 + (f0 >> 4) + 2 * T[n]) * 64 + rt * 4 + (m ? mb : ma)) * 1024 + lane * 16;
#pragma unroll
    for (int u = 0; u < 2; ++u) {
      const u32x4 o = mish8(acc[m][n], 8 * u);
      st128_sc1(ws, dst + u * 65536, o);
    }
  }
}

constexpr int XMT = 7680, XDUMP = 9 * XMT;
DI unsigned xslot_off(int sg, int odd) {
  int part, beta; qkv_slot(sg, odd, part, beta);
  return part * 2560 + beta * 512;
}

template <int NH>
DI void p1_qkv(unsigned smb, int w, int lane, int odd TSARG) {
  const int h = lane >> 5, g = lane >> 4, i = lane & 15;
  const int ma = 2 * (w & 1), T0 = 4 * (w >> 1);
  f32x16 acc[2][4] = {}; f32x4 ah[3] = {};
  const unsigned lxh = i * 128 + ((g ^ (i >> 1)) * 16);
  const int q = (lane >> 2) & 3, p = lane & 3;
  const unsigned hrow = (8 * g + q) * 512, hsw = (q << 6) | ((g & 1) << 5);
  const int hs[3] = {2 * w, 2 * w + 1, 12 + (w & 1)};
  unsigned lh[3], lw[4];
#pragma unroll
  for (int j = 0; j < 3; ++j) lh[j] = hrow + ((32 * hs[j] + 8 * p) ^ hsw);
#pragma unroll
  for (int n = 0; n < 4; ++n) { lw[n] = p1_lw(T0 + n, lane); asm volatile("" : "+v"(acc[0][n]), "+v"(acc[1][n])); }
  p1_compute<8, NH>(acc, ah, smb, p1_lx(ma, lane), p1_lx(ma + 1, lane), lw, lxh, lh TSPASS);
  const unsigned lb = smb + (1 + 2 * ma + (g & 1)) * XMT + h * 128 + i * 8;
#pragma unroll
  for (int n = 0; n < 4; ++n)
#pragma unroll
    for (int a2 = 0; a2 < 2; ++a2) {
      const int sg = 2 * (T0 + n) + a2;
      if (n == 3 && a2 == 1 && sg == 15) continue;
      const unsigned xo = lb + xslot_off(sg, odd);
#pragma unroll
      for (int a1 = 0; a1 < 2; ++a1)
#pragma unroll
        for (int m = 0; m < 2; ++m) {
          const int a = 2 * a2 + a1;
          lds_w64(xo + a1 * 256 + m * 2 * XMT, u32x2{pack_bf16(acc[m][n][4 * a], acc[m][n][4 * a + 1]), pack_bf16(acc[m][n][4 * a + 2], acc[m][n][4 * a + 3])});
        }
    }
#pragma unroll
  for (int j = 0; j < NH; ++j)
    lds_w64(smb + xslot_off(hs[j], odd) + lane * 8, u32x2{pack_bf16(ah[j][0], ah[j][1]), pack_bf16(ah[j][2], ah[j][3])});
}

DI void p1_attn(unsigned smb, int w, int lane, int rt, int hg, rsrc_t ws) {
  const int g = lane >> 4, i = lane & 15;
  const unsigned base = smb + w * XMT;
  const unsigned pa = base + lane * 8, pe = base + (5 * g + 4) * 128 + i * 8;
  const unsigned va = base + 2560 + (lane & 3) * 128 + (lane >> 2) * 8, ve = base + 2560 + (5 * (lane & 3) + 4) * 128 + (lane >> 2) * 8;
  u32x2 q[5], kp[5], kc[5], vp[5], vc[5];
#pragma unroll
  for (int t = 0; t < 5; ++t) {
    const unsigned a = t < 4 ? pa + t * 640 : pe;
    q[t] = lds_b64(a + XMT + 5120);
    kp[t] = lds_b64(a); kc[t] = lds_b64(a + XMT);
  }
#pragma unroll
  for (int t = 0; t < 5; ++t) {
    const unsigned a = t < 4 ? va + t * 640 : ve;
    vp[t] = __builtin_bit_cast(u32x2, lds_tr(a)); vc[t] = __builtin_bit_cast(u32x2, lds_tr(a + XMT));
  }
  const int par = i & 1;
  const bool c0 = (4 * g + par) <= i, c1 = (4 * g + par + 2) <= i;
  const bool first = (rt == 0) && (w == 0);
  const unsigned hm = par ? 0xffff0000u : 0x0000ffffu;
  const unsigned m_p0 = c0 ? 0u : hm, m_c0 = c0 ? hm : 0u, m_p1 = c1 ? 0u : hm, m_c1 = c1 ? hm : 0u;
  const float cs = 0.22360679774997896f * 1.44269504088896f;
  const f32x4 z4 = {0.f, 0.f, 0.f, 0.f};
  f32x4 oe = z4;
  float inv_e = 0.f;
  u32x2 op[4];
#pragma unroll
  for (int j = 0; j < 4; ++j) {
    u32x2 qe = q[4]; if (g != j) qe = u32x2{0u, 0u};
    bf16x8 qop = mk8(q[j], qe);
    f32x4 sp = MFMA16(mk8(kp[j], kp[4]), qop, z4);
    f32x4 sc = MFMA16(mk8(kc[j], kc[4]), qop, z4);
    float a0 = par ? sc[1] : sc[0], a1 = par ? sc[3] : sc[2];
    float b0 = par ? sp[1] : sp[0], b1 = par ? sp[3] : sp[2];
    if (first) { b0 = -INFINITY; b1 = -INFINITY; }
    float s0 = c0 ? a0 : b0, s1 = c1 ? a1 : b1;
    float mx = xg_max(fmaxf(s0, s1));
    float nm = -mx * cs;
    float p0 = __builtin_amdgcn_exp2f(fmaf(s0, cs, nm)), p1 = __builtin_amdgcn_exp2f(fmaf(s1, cs, nm));
    unsigned w0 = pack_bf16(p0, p0), w1 = pack_bf16(p1, p1);
    float sum = xg_sum(bf_lo(w0) + bf_lo(w1));
    float inv = __builtin_amdgcn_rcpf(sum);
    u32x4 pw = {w0 & m_p0, w1 & m_p1, w0 & m_c0, w1 & m_c1};
    bf16x8 pop = __builtin_bit_cast(bf16x8, pw);
    f32x4 o = MFMA16(mk8(vp[j], vc[j]), pop, z4);
    u32x2 ep = vp[4], ec = vc[4];
    if ((i >> 2) != j) { ep = u32x2{0u, 0u}; ec = u32x2{0u, 0u}; }
    oe = MFMA16(mk8(ep, ec), pop, oe);
    if (g == j) inv_e = inv;
    op[j] = u32x2{pack_bf16(o[0] * inv, o[1] * inv), pack_bf16(o[2] * inv, o[3] * inv)};
  }
  const int m32 = rt * 4 + (w >> 1);
  const unsigned lp = (32 * (g & 1) + 16 * (w & 1) + i) * 16;
  const unsigned d0 = ((5 * hg + (g >> 1)) * 64 + m32) * 1024 + lp;
  st128_sc1(ws, d0, u32x4{op[0][0], op[0][1], op[1][0], op[1][1]});
  st128_sc1(ws, d0 + 2 * 65536, u32x4{op[2][0], op[2][1], op[3][0], op[3][1]});
  st64_sc1(ws, ((5 * hg + 4) * 64 + m32) * 1024 + lp + (g >> 1) * 8,
           u32x2{pack_bf16(oe[0] * inv_e, oe[1] * inv_e), pack_bf16(oe[2] * inv_e, oe[3] * inv_e)});
}

template <int NM> struct Frag2 { bf16x8 a[2][NM]; s16x4 lo[2], hi[2]; };
template <int NM>
DI void p2_stage(const Frag2<NM>& C, Frag2<NM>& N, unsigned pa, unsigned pw, f32x16 (&acc)[NM]) {
#pragma unroll
  for (int k = 0; k < 2; ++k) {
#pragma unroll
    for (int m = 0; m < NM; ++m) {
      acc[m] = MFMA32(cat(C.lo[k], C.hi[k]), C.a[k][m], acc[m]);
      if (m == 0) { N.lo[k] = lds_tr(pw + k * 2048); N.hi[k] = lds_tr(pw + k * 2048 + 1024); }
      N.a[k][m] = lds_b128(pa + (k * NM + m) * 1024);
      SCHED();
    }
  }
}
constexpr int RED_OFF = 16384;
template <int NM>
DI void p2_compute(unsigned smb, int w, int lane, int m0, GAS float* gate TSARG) {
  const int kh = w >> 1, nh = w & 1, h = lane >> 5;
  constexpr int D = Ring<NM>::D, A_SLOT = Ring<NM>::A_SLOT;
  const unsigned la = smb + kh * (2 * NM * 1024) + lane * 16;
  const int q = (lane >> 2) & 3;
  const unsigned lw = smb + Ring<NM>::W_RING + kh * 4096 + (4 * h + q) * 128 + ((64 * nh + 32 * ((lane >> 4) & 1) + 8 * (lane & 3)) ^ ((q >> 1) << 6));
  f32x16 accA[NM] = {}, accB[NM] = {};
  Frag2<NM> F0, F1;
  WAIT_VM(0);
  BARRIER_RAW();
  ST(7);
  float gv[NM];
#pragma unroll
  for (int m = 0; m < NM; ++m) gv[m] = __hip_atomic_load(gate + (m0 + m) * 32 + (lane & 31), RLX_AGENT);
#pragma unroll
  for (int k = 0; k < 2; ++k) {
    F0.lo[k] = lds_tr(lw + k * 2048); F0.hi[k] = lds_tr(lw + k * 2048 + 1024);
#pragma unroll
    for (int m = 0; m < NM; ++m) F0.a[k][m] = lds_b128(la + (k * NM + m) * 1024);
  }
  unsigned ca = A_SLOT, cw = W_SLOT;
  auto adv = [&]() { ca = (ca == (D - 1) * A_SLOT) ? 0 : ca + A_SLOT; cw = (cw == (D - 1) * W_SLOT) ? 0 : cw + W_SLOT; };
#pragma unroll 1
  for (int t = 0; t < 26; t += 2) {
    BARRIER_RAW(); p2_stage<NM>(F0, F1, la + ca, lw + cw, accB); adv();
    BARRIER_RAW(); p2_stage<NM>(F1, F0, la + ca, lw + cw, accB); adv();
  }
  BARRIER_RAW(); p2_stage<NM>(F0, F1, la + ca, lw + cw, accB); adv();
  ST(8);
  BARRIER_RAW(); p2_stage<NM>(F1, F0, la + ca, lw + cw, accA); adv();
#pragma unroll 1
  for (int t = 28; t < NST - 1; t += 2) {
    BARRIER_RAW(); p2_stage<NM>(F0, F1, la + ca, lw + cw, accA); adv();
    BARRIER_RAW(); p2_stage<NM>(F1, F0, la + ca, lw + cw, accA); adv();
  }
#pragma unroll
  for (int k = 0; k < 2; ++k)
#pragma unroll
    for (int m = 0; m < NM; ++m) accA[m] = MFMA32(cat(F0.lo[k], F0.hi[k]), F0.a[k][m], accA[m]);
  ST(9);
  BARRIER();
  f32x4 keep[NM][2];
#pragma unroll
  for (int m = 0; m < NM; ++m) {
    const float hv = 1.f - gv[m];
#pragma unroll
    for (int a = 0; a < 2; ++a) {
      f32x4 lo, hi;
#pragma unroll
      for (int e = 0; e < 4; ++e) {
        lo[e] = gv[m] * accA[m][4 * a + e] + hv * accB[m][4 * a + e];
        hi[e] = gv[m] * accA[m][8 + 4 * a + e] + hv * accB[m][8 + 4 * a + e];
      }
      keep[m][a] = kh ? hi : lo;
      *(LAS f32x4*)(size_t)(smb + RED_OFF + ((w * NM + m) * 2 + a) * 1024 + lane * 16) = kh ? lo : hi;
    }
  }
  BARRIER();
#pragma unroll
  for (int m = 0; m < NM; ++m)
#pragma unroll
    for (int a = 0; a < 2; ++a) {
      const f32x4 o = keep[m][a] + *(LAS f32x4*)(size_t)(smb + RED_OFF + (((w ^ 2) * NM + m) * 2 + a) * 1024 + lane * 16);
      lds_w64(smb + (m * 32 + (lane & 31)) * 144 + nh * 64 + (2 * kh + a) * 16 + h * 8, u32x2{pack_bf16(o[0], o[1]), pack_bf16(o[2], o[3])});
    }
}

__global__ __launch_bounds__(NTHREADS, 2) void fused_kernel(const u16* __restrict__ x, const u16* __restrict__ wqkv, const u16* __restrict__ wo,
                                                            const u16* __restrict__ w1, const u16* __restrict__ b1, const u16* __restrict__ w2,
                                                            const u16* __restrict__ gw, char* __restrict__ wsp, u16* __restrict__ out) {
  extern __shared__ __attribute__((aligned(16))) char smem[];
  const unsigned smb = (unsigned)(size_t)(LAS char*)smem;
  TSDECL; ST(0);
  const int lane = threadIdx.x & 63;
  const int w = __builtin_amdgcn_readfirstlane(threadIdx.x >> 6);
  int b = blockIdx.x;
  const int is_mlp = (b >> 3) & 1;
  const int rt = ((b >> 1) & 3) * 4 + ((b >> 4) & 3);
  const int cg = (b & 1) * 4 + (b >> 6);
  const int row0 = rt * 128;
  const int ps = cg < 7 ? 7 * cg : 48, np = cg < 6 ? 7 : 6;
  int k0 = ((5 * (rt & 3)) >> 1) + ((5 * (is_mlp * 4 + (cg & 3))) >> 2);
  k0 = ROT1 == 2 ? (k0 >= 9 ? k0 - 8 : k0 + 1) : (ROT1 ? (k0 >= 10 ? k0 - 10 : k0) : 0);
  const char* wmat = is_mlp ? (const char*)w1 : (const char*)wqkv;
  const unsigned vwme = p1_vw(w, lane, is_mlp, cg, ps, np);
  {
    const int kf = ROT1 == 2 ? 0 : k0;
    const unsigned RB = is_mlp ? 3456u : 3840u;
    const char* xs = (const char*)x + kf * 128;
    const char* wsrc = wmat + kf * (64 * RB);
    const unsigned d = smb + w * 1024;
    const unsigned vx = (row0 + 8 * w + (lane >> 3)) * 1280 + (((lane & 7) ^ (4 * (w & 1) + (lane >> 4))) * 16);
    glds16(xs, vx, d + 2048);
    glds16(wsrc, vwme, d + W_OFF);
    glds16(xs + 64 * 1280, vx, d + 2048 + 8192);
    glds16(wsrc + 16 * RB, vwme, d + W_OFF + 8192);
    glds16(wsrc + 32 * RB, vwme, d + W_OFF + 16384);
    glds16(wsrc + 48 * RB, vwme, d + W_OFF + 24576);
    int tokh = row0 - 16 + 2 * w + (lane >> 5); tokh = tokh < 0 ? 0 : tokh;
    const unsigned vh = tokh * 1280 + (((((lane & 31) >> 2) ^ w) * 16) | ((lane & 3) * 4));
    asm volatile("s_mov_b32 m0, %2\n\ts_nop 0\n\tglobal_load_lds_dword %0, %1" : : "v"(vh), "s"(xs), "s"(smb + w * 256) : "memory");
  }
  if (ROT1 != 2) k0 = p1_next(k0);
  if (w >= 4) {
    {
      const char* pc;
      asm volatile("s_getpc_b64 %0" : "=s"(pc));
      const int chunk = ((b >> 3) * 4 + w) % CODE_CHUNKS;
      asm volatile("s_mov_b32 m0, %2\n\ts_mov_b32 exec_hi, 0\n\tglobal_load_lds_dword %0, %1\n\ts_mov_b32 exec_hi, -1"
                   : : "v"((unsigned)(chunk * 4096 + lane * 128)), "s"(pc), "s"(smb + 3 * K1_STAGE + w * 256) : "memory");
    }
    const unsigned vw0 = p1_vw(w - 4, lane, is_mlp, cg, ps, np);
    if (is_mlp) p1_loader<0>(smb, w - 4, lane, (const char*)x, wmat, row0, vw0, vwme, k0 TSPASS);
    else p1_loader<1>(smb, w - 4, lane, (const char*)x, wmat, row0, vw0, vwme, k0 TSPASS);
  }
  asm volatile("" : "+s"(b));
  const int xcd = b & 7, idx = b >> 3;
  int ct, m0;
  {
    const int j = idx >> 1, jj = j - 6;
    const int ctm = j < 12 ? j >> 2 : 3 + ((j - 12) >> 1), m0m = 3 * (j < 12 ? j & 3 : j & 1);
    const int ctq = j < 6 ? j >> 1 : 3 + (jj >= 5), m0q = j < 6 ? 12 + 2 * (j & 1) : 6 + 2 * (jj >= 5 ? jj - 5 : jj);
    ct = is_mlp ? ctm : ctq; m0 = is_mlp ? m0m : m0q;
  }
  int s0 = ROT2 ? ((m0 * 27) >> 4) + ct * 5 : 0;
  s0 = s0 >= 27 ? s0 - 27 : s0;
  ct += (xcd & 1) * 5; m0 += (xcd >> 1) * 16;
  const int nm = is_mlp ? 3 : 2;
  const rsrc_t ws = __builtin_amdgcn_make_buffer_rsrc(wsp, 0, WS_BYTES, 0x00020000);
  GAS u64* flags = (GAS u64*)(wsp + FLAG_OFF);
  GAS u64* epoch = (GAS u64*)(wsp + EPOCH_OFF);
  GAS float* gate = (GAS float*)(wsp + GATE_OFF);
  WLoad wl = {0u, 0u, nullptr, 0};
  u64 tag = 0;
  if (w >= 4) {
    wl = is_mlp ? p2_w_prologue<3>(smb, w - 4, lane, ct, s0, w2) : p2_w_prologue<2>(smb, w - 4, lane, ct, s0, w2);
    if (w < 6) {
      const int id = idx * 100 + (w - 4) * 50 + (lane < 50 ? lane : 49);
      const int row = id / 5;
      unsigned dm;
      asm volatile("global_load_dword %0, %1, %2" : "=v"(dm) : "v"((unsigned)(row * 1280 + ((xcd & 1) * 5 + (id - row * 5)) * 128)), "s"(wo) : "memory");
      BARRIER_RAW();
      asm volatile("s_waitcnt vmcnt(0)" : "+v"(dm) :: "memory");
    } else
    BARRIER_RAW();
    tag = __hip_atomic_load(epoch, RLX_AGENT) * 0x9E3779B97F4A7C15ull + 0x632BE59BD9B4E019ull;
    if (w >= 6) {
      if (is_mlp) {
        const int row = row0 + cg * 16 + (w - 6) * 8 + (lane >> 3);
        const char* xr = (const char*)x + row * 1280 + (lane & 7) * 16;
        const char* gr = (const char*)gw + (lane & 7) * 16;
        float s = 0.f;
#pragma unroll
        for (int t = 0; t < 10; ++t) {
          u32x4 a = *(const u32x4*)(xr + t * 128), c = *(const u32x4*)(gr + t * 128);
#pragma unroll
          for (int e = 0; e < 4; ++e) {
            const unsigned ua = a[e], uc = c[e];
            s = __builtin_amdgcn_fdot2_f32_bf16(__builtin_bit_cast(bf16x2_t, ua), __builtin_bit_cast(bf16x2_t, uc), s, false);
          }
        }
        s += __shfl_xor(s, 1); s += __shfl_xor(s, 2); s += __shfl_xor(s, 4);
        if ((lane & 7) == 0) __hip_atomic_store(gate + row, 1.f / (1.f + __expf(-s)), RLX_AGENT);
      }
    }
    ST(1); ST(2);
  } else if (is_mlp) {
    const int f0 = ps * 32;
    if (np == 7) p1_mlp<7>(smb, w, lane, f0, rt, b1, ws TSPASS);
    else p1_mlp<6>(smb, w, lane, f0, rt, b1, ws TSPASS);
  } else {
    if (w < 2) p1_qkv<3>(smb, w, lane, cg & 1 TSPASS);
    else p1_qkv<2>(smb, w, lane, cg & 1 TSPASS);
  }
  if (!is_mlp) {
    BARRIER();
    ST(3);
    p1_attn(smb, w, lane, rt, cg, ws);
  } else { ST(3); }
  ST(4);
  GAS u64* myflag = flags + rt * 16 + is_mlp * 8 + cg;
  if (is_mlp) WAIT_VM(0);
  BARRIER();
  ST(5);
  if (is_mlp && w == 6 && lane == 0) __hip_atomic_store(myflag, tag, RLX_AGENT);
  ST(6);
  if (w >= 4) {
    const int rta = m0 >> 2, rtb = (m0 + nm - 1) >> 2;
    GAS u64* fq = flags + ((lane & 8) ? rtb : rta) * 16 + (lane & 7);
    if (is_mlp) p2_loader<3>(smb, w - 4, lane, m0, s0, wl, wsp, wo, fq + 8, fq, tag, myflag);
    else p2_loader<2>(smb, w - 4, lane, m0, s0, wl, wsp, wo, fq + 8, fq, tag, myflag);
    ST(7); ST(8); ST(9);
    BARRIER_RAW();
    BARRIER_RAW();
  } else if (is_mlp) p2_compute<3>(smb, w, lane, m0, gate TSPASS);
  else p2_compute<2>(smb, w, lane, m0, gate TSPASS);
  BARRIER();
  for (int c = threadIdx.x; c < nm * 256; c += NTHREADS) {
    const int r = c >> 3, ch = c & 7;
    u32x4 v = *(LAS u32x4*)(size_t)(smb + r * 144 + ch * 16);
    { const rsrc_t ro = __builtin_amdgcn_make_buffer_rsrc(out, 0, 2048 * 1280, 0x00020000); __builtin_amdgcn_raw_buffer_store_b128(v, ro, (m0 * 32 + r) * 1280 + ct * 128 + ch * 16, 0, 16); }
  }
  if (b == 254 && w == 7) {
    for (int spin = 0; spin < SPIN_CAP; ++spin) {
      bool ok = true;
#pragma unroll
      for (int k = 0; k < 4; ++k) ok &= __hip_atomic_load(flags + k * 64 + lane, RLX_AGENT) == tag;
      if (__all(ok)) break;
      __builtin_amdgcn_s_sleep(8);
    }
    if (lane == 0) __hip_atomic_store(epoch, tag, RLX_AGENT);
  }
}

extern "C" void kernel_launch(void* const* d_in, const int* in_sizes, int n_in, void* d_out, int out_size, void* d_ws,
                              size_t ws_size, hipStream_t stream) {
  (void)hipFuncSetAttribute((const void*)fused_kernel, hipFuncAttributeMaxDynamicSharedMemorySize, LDS_BYTES);
  fused_kernel<<<256, NTHREADS, LDS_BYTES, stream>>>((const u16*)d_in[0], (const u16*)d_in[1], (const u16*)d_in[2], (const u16*)d_in[4],
                                                     (const u16*)d_in[5], (const u16*)d_in[6], (const u16*)d_in[7], (char*)d_ws, (u16*)d_out);
}
```

```cpp
#include <hip/hip_runtime.h>
#include <stdint.h>
#include <type_traits>

typedef unsigned short u16;
typedef unsigned long long u64;
typedef short bf16x8 __attribute__((ext_vector_type(8)));
typedef short s16x4 __attribute__((ext_vector_type(4)));
typedef float f32x4 __attribute__((ext_vector_type(4)));
typedef float f32x16 __attribute__((ext_vector_type(16)));
typedef float f32x2 __attribute__((ext_vector_type(2)));
typedef unsigned u32x4 __attribute__((ext_vector_type(4)));
typedef unsigned u32x2 __attribute__((ext_vector_type(2)));
typedef unsigned u32x16 __attribute__((ext_vector_type(16)));
typedef __bf16 bf16x2_t __attribute__((ext_vector_type(2)));
#define LAS __attribute__((address_space(3)))
#define GAS __attribute__((address_space(1)))
#define DI __device__ __forceinline__

#define MFMA16(a, b, c) __builtin_amdgcn_mfma_f32_16x16x32_bf16((a), (b), (c), 0, 0, 0)
#define MFMA32(a, b, c) __builtin_amdgcn_mfma_f32_32x32x16_bf16((a), (b), (c), 0, 0, 0)
#define WAIT_VM(n) asm volatile("s_waitcnt vmcnt(" #n ")" ::: "memory")
#define WAIT_VMI(n) asm volatile("s_waitcnt vmcnt(%0)" ::"i"(n) : "memory")
#define BARRIER() do { asm volatile("s_waitcnt lgkmcnt(0)" ::: "memory"); __builtin_amdgcn_s_barrier(); asm volatile("" ::: "memory"); } while (0)
#define BARRIER_RAW() do { asm volatile("" ::: "memory"); __builtin_amdgcn_s_barrier(); asm volatile("" ::: "memory"); } while (0)
#define SCHED() __builtin_amdgcn_sched_barrier(0)
#define RLX_AGENT __ATOMIC_RELAXED, __HIP_MEMORY_SCOPE_AGENT

#define TSARG
#define TSPASS
#define TSDECL
#define ST(id)
#define TSOUT()

constexpr int NTHREADS = 512;
constexpr int NKAP = 148;
constexpr int W_OFF = 18432;
constexpr int K1_STAGE = 51200;
constexpr unsigned MID_BYTES = NKAP * 64 * 1024;
constexpr unsigned GATE_OFF = MID_BYTES, FLAG_OFF = GATE_OFF + 8192, EPOCH_OFF = FLAG_OFF + 2048, WS_BYTES = EPOCH_OFF + 128;
constexpr int LDS_BYTES = 163840;
constexpr int W_SLOT = 8192;
#define WPQ 6
#define WP0 2
template <int NM> struct Ring {
  static constexpr int D = NM == 3 ? 8 : 10;
  static constexpr int A_SLOT = NM * 4096;
  static constexpr int W_RING = D * A_SLOT;
  static constexpr int WP = NM == 3 ? D - 1 : WPQ;
};
constexpr int NST = NKAP / 4;
constexpr int SPIN_CAP = 1 << 22;
constexpr int PFN = 1;
constexpr int CODE_CHUNKS = 11;

DI unsigned pack_bf16(float lo, float hi) {
  f32x2 f = {lo, hi};
  return __builtin_bit_cast(unsigned, __builtin_convertvector(f, bf16x2_t));
}
DI float bf_lo(unsigned u) { return __uint_as_float(u << 16); }
DI float bf_hi(unsigned u) { return __uint_as_float(u & 0xffff0000u); }
DI void glds16(const char* sbase, unsigned voff, unsigned dst) {
  asm volatile("s_mov_b32 m0, %2\n\ts_nop 0\n\tglobal_load_lds_dwordx4 %0, %1" : : "v"(voff), "s"(sbase), "s"(dst) : "memory");
}
DI void glds16_sc1(const char* sbase, unsigned voff, unsigned dst) {
  asm volatile("s_mov_b32 m0, %2\n\ts_nop 0\n\tglobal_load_lds_dwordx4 %0, %1 sc1" : : "v"(voff), "s"(sbase), "s"(dst) : "memory");
}
DI void glds16_lo(const char* sbase, unsigned voff, unsigned dst) {
  asm volatile("s_mov_b32 m0, %2\n\ts_mov_b32 exec_hi, 0\n\tglobal_load_lds_dwordx4 %0, %1\n\ts_mov_b32 exec_hi, -1" : : "v"(voff), "s"(sbase), "s"(dst) : "memory");
}
DI bf16x8 lds_b128(unsigned a) { return *(LAS bf16x8*)(size_t)a; }
DI s16x4 lds_tr(unsigned a) { return __builtin_amdgcn_ds_read_tr16_b64_v4i16((LAS s16x4*)(size_t)a); }
DI u32x2 lds_b64(unsigned a) { return *(LAS u32x2*)(size_t)a; }
DI void lds_w64(unsigned a, u32x2 v) { *(LAS u32x2*)(size_t)a = v; }
DI bf16x8 cat(s16x4 lo, s16x4 hi) { return __builtin_shufflevector(lo, hi, 0, 1, 2, 3, 4, 5, 6, 7); }
DI bf16x8 mk8(u32x2 a, u32x2 b) {
  u32x4 t = {a[0], a[1], b[0], b[1]};
  return __builtin_bit_cast(bf16x8, t);
}
typedef __amdgpu_buffer_rsrc_t rsrc_t;
#define STAUX 16
DI void st128_sc1(rsrc_t r, unsigned off, u32x4 v) { __builtin_amdgcn_raw_buffer_store_b128(v, r, off, 0, STAUX); }
DI void st64_sc1(rsrc_t r, unsigned off, u32x2 v) { __builtin_amdgcn_raw_buffer_store_b64(v, r, off, 0, 16); }

DI u32x4 mish8(const f32x16& v, int o) {
  f32x2 h[4], z[4], e[4], p[4], d[4], r[4];
  const f32x2 c1 = {1.44269504088896f / 160.f, 1.44269504088896f / 160.f}, c0 = {129.f / 160.f, 129.f / 160.f};
#pragma unroll
  for (int k = 0; k < 4; ++k) { h[k] = f32x2{v[o + 2 * k], v[o + 2 * k + 1]}; asm("v_pk_fma_f32 %0, %1, %2, %3 clamp" : "=v"(z[k]) : "v"(h[k]), "v"(c1), "v"(c0)); }
#pragma unroll
  for (int k = 0; k < 4; ++k) z[k] = z[k] * 160.f - 129.f;
  SCHED();
#pragma unroll
  for (int k = 0; k < 4; ++k) e[k] = f32x2{__builtin_amdgcn_exp2f(z[k][0]), __builtin_amdgcn_exp2f(z[k][1])};
  SCHED();
#pragma unroll
  for (int k = 0; k < 4; ++k) p[k] = e[k] + 2.f;
#pragma unroll
  for (int k = 0; k < 4; ++k) d[k] = e[k] * p[k] + 2.f;
  SCHED();
#pragma unroll
  for (int k = 0; k < 4; ++k) r[k] = f32x2{__builtin_amdgcn_rcpf(d[k][0]), __builtin_amdgcn_rcpf(d[k][1])};
  SCHED();
#pragma unroll
  for (int k = 0; k < 4; ++k) h[k] = h[k] * e[k];
#pragma unroll
  for (int k = 0; k < 4; ++k) h[k] = h[k] * p[k];
#pragma unroll
  for (int k = 0; k < 4; ++k) h[k] = h[k] * r[k];
  u32x4 o4;
#pragma unroll
  for (int k = 0; k < 4; ++k) o4[k] = pack_bf16(h[k][0], h[k][1]);
  SCHED();
  return o4;
}
DI float xg_max(float v) {
  unsigned u = __float_as_uint(v);
  auto r = __builtin_amdgcn_permlane32_swap(u, u, false, false);
  v = fmaxf(__uint_as_float(r[0]), __uint_as_float(r[1]));
  u = __float_as_uint(v);
  auto r2 = __builtin_amdgcn_permlane16_swap(u, u, false, false);
  return fmaxf(__uint_as_float(r2[0]), __uint_as_float(r2[1]));
}
DI float xg_sum(float v) {
  unsigned u = __float_as_uint(v);
  auto r = __builtin_amdgcn_permlane32_swap(u, u, false, false);
  v = __uint_as_float(r[0]) + __uint_as_float(r[1]);
  u = __float_as_uint(v);
  auto r2 = __builtin_amdgcn_permlane16_swap(u, u, false, false);
  return __uint_as_float(r2[0]) + __uint_as_float(r2[1]);
}

#define RAMP0 2
template <int D>
constexpr int p2_issued(int t) {
  int n = RAMP0;
  for (int i = 0; i <= t; ++i) { n += 2; if (n > i + D - 1) n = i + D - 1; }
  return n > NST ? NST : n;
}
template <int NM, int D>
constexpr int p2_allowed(int t) {
  int n = 0;
  for (int st = t + 2; st < p2_issued<D>(t); ++st) n += st < Ring<NM>::WP ? NM : NM + 2;
  return n;
}
template <int I, int N, class F>
DI void static_for(F&& f) {
  if constexpr (I < N) { f(std::integral_constant<int, I>{}); static_for<I + 1, N>(f); }
}
struct WLoad { unsigned voffn, voffe; const char* src; int st; };
DI WLoad p2_w_init(int l, int lane, int ct, int s0, const u16* __restrict__ w2) {
  const int r8 = lane >> 3;
  const unsigned colb = ct * 128 + (((lane & 7) ^ (((r8 >> 1) & 1) << 2)) * 16);
  WLoad s;
  s.voffn = r8 * 1280 + colb;
  s.voffe = (20 * (r8 >> 2) + (r8 & 3)) * 1280 + colb;
  s.src = (const char*)w2 + (16 * l) * 1280;
  s.st = s0;
  return s;
}
DI void p2_w_pre(WLoad& s, unsigned d, int t0, int t1) {
#pragma unroll 1
  for (int t = t0; t < t1; ++t) {
    const char* p = s.src + s.st * (64 * 1280);
    glds16(p, s.voffn, d + t * W_SLOT);
    glds16(p + 8 * 1280, s.voffn, d + t * W_SLOT + 1024);
    s.st = s.st == 26 ? 0 : s.st + 1;
  }
}
DI void spin_flags(GAS u64* fp, u64 tag) {
#pragma unroll 1
  for (int spin = 0; spin < SPIN_CAP; ++spin) {
    if (__all(__hip_atomic_load(fp, RLX_AGENT) == tag)) break;
    __builtin_amdgcn_s_sleep(2);
  }
  asm volatile("" ::: "memory");
}
template <int NM>
DI void p2_loader(unsigned smb, int l, int lane, int m0, int sa, WLoad& ws, const char* __restrict__ mid, const u16* __restrict__ wo,
                  GAS u64* fpm, GAS u64* fpq, u64 tag, GAS u64* myflag) {
  constexpr int D = Ring<NM>::D, A_SLOT = Ring<NM>::A_SLOT;
  const unsigned lane16 = lane * 16;
  const char* src = mid + (size_t)((40 + l) * 64 + m0) * 1024;
  const unsigned da = smb + l * (NM * 1024), dw = smb + Ring<NM>::W_RING + l * 2048;
  auto issue_a = [&](unsigned slot) {
    const char* p = src + (size_t)sa * (4 * 65536);
#pragma unroll
    for (int m = 0; m < NM; ++m) glds16_sc1(p + m * 1024, lane16, da + slot * A_SLOT + m * 1024);
    sa = sa == 26 ? 0 : sa + 1;
  };
  spin_flags(fpm, tag);
#pragma unroll
  for (int t = 0; t < RAMP0; ++t) issue_a(t);
  WAIT_VMI((p2_allowed<NM, D>(-1)));
  BARRIER_RAW();
  if (NM == 2 && l == 2 && lane == 0) {
    asm volatile("global_store_dwordx2 %0, %1, off sc1" : : "v"(myflag), "v"(tag) : "memory");
  }
  unsigned ps = Ring<NM>::WP;
  u64 fq = 0;
  auto issue_act = [&]() {
    const char* p = ws.src + sa * (64 * 1280);
    glds16(p, ws.voffn, dw + ps * W_SLOT);
    glds16(p + 8 * 1280, ws.voffn, dw + ps * W_SLOT + 1024);
    issue_a(ps);
    ps = (ps == D - 1) ? 0 : ps + 1;
  };
  auto issue_range = [&](auto c0, auto c1) {
    static_for<c0.value, c1.value>([&](auto sc) { if constexpr (sc.value < Ring<NM>::WP) issue_a(sc.value); else issue_act(); });
  };
  issue_range(std::integral_constant<int, p2_issued<D>(-1)>{}, std::integral_constant<int, p2_issued<D>(0)>{});
  static_for<0, D - 3>([&](auto tc) {
    WAIT_VMI((p2_allowed<NM, D>(tc.value))); BARRIER_RAW();
    issue_range(std::integral_constant<int, p2_issued<D>(tc.value)>{}, std::integral_constant<int, p2_issued<D>(tc.value + 1)>{});
  });
  static_assert(p2_issued<D>(D - 3) == 2 * D - 4, "ring must be full when the steady state starts");
#pragma unroll 1
  for (int t = D - 3; t + D - 1 <= 24; ++t) { WAIT_VMI((NM + 2) * (D - 3)); BARRIER_RAW(); issue_act(); }
  asm volatile("global_load_dwordx2 %0, %1, off sc1" : "=v"(fq) : "v"(fpq) : "memory");
  WAIT_VMI((NM + 2) * (D - 3)); BARRIER_RAW(); issue_act();
  WAIT_VMI((NM + 2) * (D - 3)); BARRIER_RAW(); issue_act();
  asm volatile("s_waitcnt vmcnt(%1)" : "+v"(fq) : "i"(2 * (NM + 2)) : "memory");
  if (!__all(fq == tag)) spin_flags(fpq, tag);
  src = mid + (size_t)(l * 64 + m0) * 1024; sa = 0;
  int u = l;
  const char* won = (const char*)wo;
#pragma unroll 1
  for (int j = 0; j < 10; ++j) {
    WAIT_VMI((NM + 2) * (D - 3)); BARRIER_RAW();
    const int r0 = u < 4 ? 40 * (u >> 1) + 8 * (u & 1) : 16, dr = u < 4 ? 20 : 40;
    const unsigned vo = u < 4 ? ws.voffn : ws.voffe;
    glds16(won + r0 * 1280, vo, dw + ps * W_SLOT);
    glds16(won + (r0 + dr) * 1280, vo, dw + ps * W_SLOT + 1024);
    won += u >= 1 ? 80 * 1280 : 0;
    u = u >= 1 ? u - 1 : 4;
    issue_a(ps);
    ps = (ps == D - 1) ? 0 : ps + 1;
  }
  static_for<NST - D + 1, NST - 1>([&](auto tc) { WAIT_VMI((p2_allowed<NM, D>(tc.value))); BARRIER_RAW(); });
}

DI void qkv_slot(int sg, int odd, int& P, int& beta) {
  if (sg < 12) { P = sg >> 2; beta = (sg & 3) + odd; }
  else { P = sg < 15 ? sg - 12 : 2; beta = odd ? 0 : 4; }
}
DI unsigned p1_vw(int v, int lane, int is_mlp, int cg, int ps, int np) {
  const int hi = lane >> 5;
  const int c = (lane & 31) ^ ((((2 * v + hi) & 3) << 2) | ((v >> 2) << 1));
  const int sg = c >> 1, odd = cg & 1;
  const int P = sg < 12 ? sg >> 2 : (sg < 15 ? sg - 12 : 2);
  const int beta = sg < 12 ? (sg & 3) + odd : (odd ? 0 : 4);
  const unsigned colq = (P == 2 ? 0 : (P + 1) * 1280) + cg * 160 + beta * 32 + (c & 1) * 16;
  const unsigned colm = ps * 64 + (c < 4 * np ? c : 4 * np - 1) * 16;
  return (2 * v + hi) * (is_mlp ? 3456u : 3840u) + (is_mlp ? colm : colq);
}
#define ROT1 2
#define ROT2 1
#define PRE1 5
DI int p1_next(int kk) { return kk == 9 ? (ROT1 == 2 ? 1 : 0) : kk + 1; }
struct Side { const char* wsp; const char* w2; const char* wo; int b, rt, cg, ps, np; };
#define PREW 0
#define PRET 0
template <int QKV>
DI void p1_loader(unsigned smb, int L, int lane, const char* __restrict__ x, const char* __restrict__ wmat, int row0, unsigned vw0, unsigned vw1, int kk,
                  const Side& sd, u64& ep TSARG) {
  constexpr unsigned RB = QKV ? 3840 : 3456;
  constexpr int NL = QKV ? 13 : 12;
  const unsigned vx = (row0 + 8 * L + (lane >> 3)) * 1280 + (((lane & 7) ^ (4 * (L & 1) + (lane >> 4))) * 16);
  const int hr = 4 * L + ((lane >> 3) & 3);
  int tokh = row0 - 16 + hr; tokh = tokh < 0 ? 0 : tokh;
  const unsigned vh = tokh * 1280 + (((lane & 7) ^ ((hr >> 1) & 7)) * 16);
  auto piece = [&](int p, unsigned buf) {
    const unsigned d = smb + buf + L * 1024;
    const char* xs = x + kk * 128;
    const char* wsrc = wmat + kk * (64 * RB);
    if (p < 8) { if (p & 1) glds16(xs + (p >> 1) * (32 * 1280), vx, d + 2048 + (p >> 1) * 4096);
                 else glds16(wsrc + (p >> 2) * (16 * RB), (p & 2) ? vw1 : vw0, d + W_OFF + (p >> 1) * 4096); }
    else if (p < 12) glds16(wsrc + ((p - 4) >> 1) * (16 * RB), (p & 1) ? vw1 : vw0, d + W_OFF + (p - 4) * 4096);
    else glds16_lo(xs, vh, smb + buf + L * 512);
  };
  auto issue = [&](unsigned buf) {
#pragma unroll
    for (int p = 0; p < NL; ++p) piece(p, buf);
    kk = p1_next(kk);
  };
#pragma unroll
  for (int p = 0; p < PRE1; ++p) piece(p, K1_STAGE);
  WAIT_VMI(PRE1 + PFN);
  BARRIER_RAW();
  asm volatile("global_load_dwordx2 %0, %1, %2 sc1" : "=v"(ep) : "v"(EPOCH_OFF), "s"(sd.wsp) : "memory");
#pragma unroll
  for (int p = PRE1; p < NL; ++p) piece(p, K1_STAGE);
  kk = p1_next(kk);
  issue(2 * K1_STAGE);
  unsigned nb = 0;
#pragma unroll 1
  for (int t = 0; t < 7; ++t) {
    WAIT_VMI(NL);
    BARRIER_RAW();
    issue(nb);
    nb = (nb == 2 * K1_STAGE) ? 0 : nb + K1_STAGE;
  }
  constexpr int NPW = QKV ? 5 : 14, NT = PRET == 2 ? 3 : 1, XS = (PREW ? NPW : 0) + (PRET ? NT : 0);
  if (PRET) {
    const unsigned dummy = smb + 3 * K1_STAGE + (4 + L) * 256;
#pragma unroll
    for (int k = 0; k < NT; ++k) {
      int id = k * 4096 + ((sd.b >> 3) * 4 + L) * 32 + (lane & 31);
      id = id < (PRET == 2 ? 11840 : 3200) ? id : 3199;
      const int row = id / 5;
      const long long off = (row < 640 ? (long long)row * 1280 : (sd.w2 - sd.wo) + (long long)(row - 640) * 1280) + ((sd.b & 1) * 5 + id - row * 5) * 128;
      asm volatile("s_mov_b32 m0, %1\n\ts_mov_b32 exec_hi, 0\n\tglobal_load_lds_dword %0, off\n\ts_mov_b32 exec_hi, -1"
                   : : "v"(sd.wo + off), "s"(dummy) : "memory");
    }
  }
  if (PREW) {
    unsigned pw = ((QKV ? 5 * sd.cg : 40 + 2 * sd.ps) * 64 + sd.rt * 4 + L) * 1024 + lane * 16;
    const unsigned pwend = pw + ((QKV ? 5 : 2 * sd.np) - 1) * 65536;
    const u32x4 zero4 = {0u, 0u, 0u, 0u};
#pragma unroll
    for (int k = 0; k < NPW; ++k) {
      asm volatile("global_store_dwordx4 %0, %1, %2 sc1\n\ts_nop 1" : : "v"(pw), "v"(zero4), "s"(sd.wsp) : "memory");
      pw = pw < pwend ? pw + 65536 : pw;
    }
  }
  WAIT_VMI(NL + XS);
  BARRIER_RAW();
  WAIT_VMI(XS);
  BARRIER_RAW();
}

struct Frag { bf16x8 x[2]; s16x4 lo[4], hi[4]; };
struct HFrag { bf16x8 x; s16x4 lo[3], hi[3]; };

template <int NMM>
DI void p1_read(Frag& F, int idx, unsigned ax0, unsigned ax1, const unsigned (&aw)[4], int s) {
  if (idx == 0) F.x[0] = lds_b128(ax0);
  else if (idx == 3) F.x[1] = lds_b128(ax1);
  else if (idx < (NMM > 6 ? 10 : 8)) {
    const int k = idx < 3 ? idx - 1 : idx - 2;
    if ((k & 1) == 0) F.lo[k >> 1] = lds_tr(aw[k >> 1] + W_OFF + s * 8192);
    else F.hi[k >> 1] = lds_tr(aw[k >> 1] + W_OFF + s * 8192 + 2048);
  }
}

template <int NMM, int NH>
DI void p1_compute(f32x16 (&acc)[2][4], f32x4 (&ah)[3], unsigned smb, unsigned lx0, unsigned lx1, const unsigned (&lw)[4],
                   unsigned lxh, const unsigned (&lh)[3] TSARG) {
  Frag F[2];
  HFrag H;
  unsigned aw[4];
#pragma unroll
  for (int n = 0; n < 4; ++n) aw[n] = smb + lw[n];
  WAIT_VMI(NH ? 0 : (NMM > 6 ? 4 : 3));
  BARRIER_RAW();
  ST(1);
#pragma unroll
  for (int i = 0; i < 10; ++i) p1_read<NMM>(F[0], i, smb + lx0, smb + lx1, aw, 0);
  unsigned cur = smb;
#pragma unroll 1
  for (int t = 0; t < 10; ++t) {
    const unsigned nxt = (cur == smb + 2 * K1_STAGE) ? smb : cur + K1_STAGE;
#pragma unroll
    for (int s = 0; s < 4; ++s) {
      const Frag& C = F[s & 1];
      Frag& N = F[(s + 1) & 1];
      const unsigned nb = s == 3 ? nxt : cur;
      const int ns = (s + 1) & 3;
      const unsigned ax0 = nb + (lx0 ^ (32 * ns)), ax1 = nb + (lx1 ^ (32 * ns));
#pragma unroll
      for (int mm = 0; mm < NMM; ++mm) {
        const int n = mm >> 1, m = mm & 1;
        acc[m][n] = MFMA32(cat(C.lo[n], C.hi[n]), C.x[m], acc[m][n]);
        if (s == 3 && mm == 0) {
          SCHED();
          BARRIER();
#pragma unroll
          for (int q = 0; q < 4; ++q) aw[q] = nxt + lw[q];
        }
        p1_read<NMM>(N, 2 * mm, ax0, ax1, aw, ns); p1_read<NMM>(N, 2 * mm + 1, ax0, ax1, aw, ns);
        if (NH) {
          if (mm == 4 && (s == 1 || s == 2)) {
#pragma unroll
            for (int j = 0; j < NH; ++j) ah[j] = MFMA16(cat(H.lo[j], H.hi[j]), H.x, ah[j]);
          }
          if (mm >= 5 && s < 2) {
            if (mm == 5) H.x = lds_b128(cur + (lxh ^ (64 * s)));
            if (mm - 5 < NH) { H.lo[mm - 5] = lds_tr(cur + lh[mm - 5] + W_OFF + s * 16384); H.hi[mm - 5] = lds_tr(cur + lh[mm - 5] + W_OFF + s * 16384 + 2048); }
          }
        }
        SCHED();
      }
    }
    cur = nxt;
  }
  ST(2);
}

DI unsigned p1_lx(int m32, int lane) {
  const int r = lane & 31;
  return (16 + 32 * m32 + r) * 128 + ((((lane >> 5)) ^ ((r >> 1) & 7)) * 16);
}
DI unsigned p1_lw(int T, int lane) {
  const int h = lane >> 5, q = (lane >> 2) & 3, p = lane & 3, cb = (lane >> 4) & 1;
  return (8 * h + q) * 512 + ((64 * T + 32 * cb + 8 * p) ^ ((q << 6) | (h << 5)));
}

template <int NMM>
DI void p1_mlp(unsigned smb, int w, int lane, int f0, int rt, const u16* __restrict__ b1, rsrc_t ws TSARG) {
  const int h = lane >> 5;
  const int nh = w >> 1;
  const int ma = 2 * (w & 1) + nh, mb = ma ^ 1;
  const int T[4] = {3 * nh, 3 * nh + 1, 3 * nh + 2, 6};
  f32x16 acc[2][4]; f32x4 ah[3];
  unsigned lw[4];
  unsigned bv[4];
#pragma unroll
  for (int n = 0; n < 4; ++n) {
    lw[n] = p1_lw(T[n], lane);
    if (n < 3 || NMM > 6)
      asm volatile("global_load_ushort %0, %1, %2" : "=v"(bv[n]) : "v"((unsigned)((f0 + 32 * T[n] + (lane & 31)) * 2)), "s"(b1) : "memory");
    acc[0][n] = f32x16{}; acc[1][n] = f32x16{};
    asm volatile("" : "+v"(acc[0][n]), "+v"(acc[1][n]));
  }
  const unsigned lh[3] = {0u, 0u, 0u};
  p1_compute<NMM, 0>(acc, ah, smb, p1_lx(ma, lane), p1_lx(mb, lane), lw, 0u, lh TSPASS);
  {
    WAIT_VM(0);
    const u32x4 one = {h ? 0u : 0x3f80u, 0u, 0u, 0u};
#pragma unroll
    for (int mm = 0; mm < NMM; ++mm) {
      const int n = mm >> 1, m = mm & 1;
      asm volatile("" : "+v"(bv[n]));
      const u32x4 bf = {h ? 0u : bv[n], 0u, 0u, 0u};
      acc[m][n] = MFMA32(__builtin_bit_cast(bf16x8, bf), __builtin_bit_cast(bf16x8, one), acc[m][n]);
    }
  }
  asm volatile("s_nop 15\n\ts_nop 15" : "+v"(acc[0][0]), "+v"(acc[1][0]), "+v"(acc[0][1]), "+v"(acc[1][1]), "+v"(acc[0][2]), "+v"(acc[1][2]));
  if (NMM > 6) asm volatile("" : "+v"(acc[0][3]));
#pragma unroll
  for (int mm = 0; mm < NMM; ++mm) {
    const int n = mm >> 1, m = mm & 1;
    const unsigned dst = ((40 + (f0 >> 4) + 2 * T[n]) * 64 + rt * 4 + (m ? mb : ma)) * 1024 + lane * 16;
#pragma unroll
    for (int u = 0; u < 2; ++u) {
      const u32x4 o = mish8(acc[m][n], 8 * u);
      st128_sc1(ws, dst + u * 65536, o);
    }
  }
}

constexpr int XMT = 7680, XDUMP = 9 * XMT;
DI unsigned xslot_off(int sg, int odd) {
  int part, beta; qkv_slot(sg, odd, part, beta);
  return part * 2560 + beta * 512;
}

template <int NH>
DI void p1_qkv(unsigned smb, int w, int lane, int odd TSARG) {
  const int h = lane >> 5, g = lane >> 4, i = lane & 15;
  const int ma = 2 * (w & 1), T0 = 4 * (w >> 1);
  f32x16 acc[2][4] = {}; f32x4 ah[3] = {};
  const unsigned lxh = i * 128 + ((g ^ (i >> 1)) * 16);
  const int q = (lane >> 2) & 3, p = lane & 3;
  const unsigned hrow = (8 * g + q) * 512, hsw = (q << 6) | ((g & 1) << 5);
  const int hs[3] = {2 * w, 2 * w + 1, 12 + (w & 1)};
  unsigned lh[3], lw[4];
#pragma unroll
  for (int j = 0; j < 3; ++j) lh[j] = hrow + ((32 * hs[j] + 8 * p) ^ hsw);
#pragma unroll
  for (int n = 0; n < 4; ++n) { lw[n] = p1_lw(T0 + n, lane); asm volatile("" : "+v"(acc[0][n]), "+v"(acc[1][n])); }
  p1_compute<8, NH>(acc, ah, smb, p1_lx(ma, lane), p1_lx(ma + 1, lane), lw, lxh, lh TSPASS);
  const unsigned lb = smb + (1 + 2 * ma + (g & 1)) * XMT + h * 128 + i * 8;
#pragma unroll
  for (int n = 0; n < 4; ++n)
#pragma unroll
    for (int a2 = 0; a2 < 2; ++a2) {
      const int sg = 2 * (T0 + n) + a2;
      if (n == 3 && a2 == 1 && sg == 15) continue;
      const unsigned xo = lb + xslot_off(sg, odd);
#pragma unroll
      for (int a1 = 0; a1 < 2; ++a1)
#pragma unroll
        for (int m = 0; m < 2; ++m) {
          const int a = 2 * a2 + a1;
          lds_w64(xo + a1 * 256 + m * 2 * XMT, u32x2{pack_bf16(acc[m][n][4 * a], acc[m][n][4 * a + 1]), pack_bf16(acc[m][n][4 * a + 2], acc[m][n][4 * a + 3])});
        }
    }
#pragma unroll
  for (int j = 0; j < NH; ++j)
    lds_w64(smb + xslot_off(hs[j], odd) + lane * 8, u32x2{pack_bf16(ah[j][0], ah[j][1]), pack_bf16(ah[j][2], ah[j][3])});
}

DI void p1_attn(unsigned smb, int w, int lane, int rt, int hg, rsrc_t ws) {
  const int g = lane >> 4, i = lane & 15;
  const unsigned base = smb + w * XMT;
  const unsigned pa = base + lane * 8, pe = base + (5 * g + 4) * 128 + i * 8;
  const unsigned va = base + 2560 + (lane & 3) * 128 + (lane >> 2) * 8, ve = base + 2560 + (5 * (lane & 3) + 4) * 128 + (lane >> 2) * 8;
  u32x2 q[5], kp[5], kc[5], vp[5], vc[5];
#pragma unroll
  for (int t = 0; t < 5; ++t) {
    const unsigned a = t < 4 ? pa + t * 640 : pe;
    q[t] = lds_b64(a + XMT + 5120);
    kp[t] = lds_b64(a); kc[t] = lds_b64(a + XMT);
  }
#pragma unroll
  for (int t = 0; t < 5; ++t) {
    const unsigned a = t < 4 ? va + t * 640 : ve;
    vp[t] = __builtin_bit_cast(u32x2, lds_tr(a)); vc[t] = __builtin_bit_cast(u32x2, lds_tr(a + XMT));
  }
  const int par = i & 1;
  const bool c0 = (4 * g + par) <= i, c1 = (4 * g + par + 2) <= i;
  const bool first = (rt == 0) && (w == 0);
  const unsigned hm = par ? 0xffff0000u : 0x0000ffffu;
  const unsigned m_p0 = c0 ? 0u : hm, m_c0 = c0 ? hm : 0u, m_p1 = c1 ? 0u : hm, m_c1 = c1 ? hm : 0u;
  const float cs = 0.22360679774997896f * 1.44269504088896f;
  const f32x4 z4 = {0.f, 0.f, 0.f, 0.f};
  f32x4 oe = z4;
  float inv_e = 0.f;
  u32x2 op[4];
#pragma unroll
  for (int j = 0; j < 4; ++j) {
    u32x2 qe = q[4]; if (g != j) qe = u32x2{0u, 0u};
    bf16x8 qop = mk8(q[j], qe);
    f32x4 sp = MFMA16(mk8(kp[j], kp[4]), qop, z4);
    f32x4 sc = MFMA16(mk8(kc[j], kc[4]), qop, z4);
    float a0 = par ? sc[1] : sc[0], a1 = par ? sc[3] : sc[2];
    float b0 = par ? sp[1] : sp[0], b1 = par ? sp[3] : sp[2];
    if (first) { b0 = -INFINITY; b1 = -INFINITY; }
    float s0 = c0 ? a0 : b0, s1 = c1 ? a1 : b1;
    float mx = xg_max(fmaxf(s0, s1));
    float nm = -mx * cs;
    float p0 = __builtin_amdgcn_exp2f(fmaf(s0, cs, nm)), p1 = __builtin_amdgcn_exp2f(fmaf(s1, cs, nm));
    unsigned w0 = pack_bf16(p0, p0), w1 = pack_bf16(p1, p1);
    float sum = xg_sum(bf_lo(w0) + bf_lo(w1));
    float inv = __builtin_amdgcn_rcpf(sum);
    u32x4 pw = {w0 & m_p0, w1 & m_p1, w0 & m_c0, w1 & m_c1};
    bf16x8 pop = __builtin_bit_cast(bf16x8, pw);
    f32x4 o = MFMA16(mk8(vp[j], vc[j]), pop, z4);
    u32x2 ep = vp[4], ec = vc[4];
    if ((i >> 2) != j) { ep = u32x2{0u, 0u}; ec = u32x2{0u, 0u}; }
    oe = MFMA16(mk8(ep, ec), pop, oe);
    if (g == j) inv_e = inv;
    op[j] = u32x2{pack_bf16(o[0] * inv, o[1] * inv), pack_bf16(o[2] * inv, o[3] * inv)};
  }
  const int m32 = rt * 4 + (w >> 1);
  const unsigned lp = (32 * (g & 1) + 16 * (w & 1) + i) * 16;
  const unsigned d0 = ((5 * hg + (g >> 1)) * 64 + m32) * 1024 + lp;
  st128_sc1(ws, d0, u32x4{op[0][0], op[0][1], op[1][0], op[1][1]});
  st128_sc1(ws, d0 + 2 * 65536, u32x4{op[2][0], op[2][1], op[3][0], op[3][1]});
  st64_sc1(ws, ((5 * hg + 4) * 64 + m32) * 1024 + lp + (g >> 1) * 8,
           u32x2{pack_bf16(oe[0] * inv_e, oe[1] * inv_e), pack_bf16(oe[2] * inv_e, oe[3] * inv_e)});
}

template <int NM> struct Frag2 { bf16x8 a[2][NM]; s16x4 lo[2], hi[2]; };
template <int NM>
DI void p2_stage(const Frag2<NM>& C, Frag2<NM>& N, unsigned pa, unsigned pw, f32x16 (&acc)[NM]) {
#pragma unroll
  for (int k = 0; k < 2; ++k) {
#pragma unroll
    for (int m = 0; m < NM; ++m) {
      acc[m] = MFMA32(cat(C.lo[k], C.hi[k]), C.a[k][m], acc[m]);
      if (m == 0) { N.lo[k] = lds_tr(pw + k * 2048); N.hi[k] = lds_tr(pw + k * 2048 + 1024); }
      N.a[k][m] = lds_b128(pa + (k * NM + m) * 1024);
      SCHED();
    }
  }
}
constexpr int RED_OFF = 16384;
template <int NM>
DI void p2_compute(unsigned smb, int w, int lane, int m0, GAS float* gate, char* __restrict__ outp TSARG) {
  const int kh = w >> 1, nh = w & 1, h = lane >> 5;
  constexpr int D = Ring<NM>::D, A_SLOT = Ring<NM>::A_SLOT;
  const unsigned la = smb + kh * (2 * NM * 1024) + lane * 16;
  const int q = (lane >> 2) & 3;
  const unsigned lw = smb + Ring<NM>::W_RING + kh * 4096 + (4 * h + q) * 128 + ((64 * nh + 32 * ((lane >> 4) & 1) + 8 * (lane & 3)) ^ ((q >> 1) << 6));
  f32x16 accA[NM] = {}, accB[NM] = {};
  Frag2<NM> F0, F1;
  WAIT_VM(0);
  BARRIER_RAW();
  ST(7);
  float gv[NM];
#pragma unroll
  for (int m = 0; m < NM; ++m) gv[m] = __hip_atomic_load(gate + (m0 + m) * 32 + (lane & 31), RLX_AGENT);
#pragma unroll
  for (int k = 0; k < 2; ++k) {
    F0.lo[k] = lds_tr(lw + k * 2048); F0.hi[k] = lds_tr(lw + k * 2048 + 1024);
#pragma unroll
    for (int m = 0; m < NM; ++m) F0.a[k][m] = lds_b128(la + (k * NM + m) * 1024);
  }
  unsigned ca = A_SLOT, cw = W_SLOT;
  auto adv = [&]() { ca = (ca == (D - 1) * A_SLOT) ? 0 : ca + A_SLOT; cw = (cw == (D - 1) * W_SLOT) ? 0 : cw + W_SLOT; };
#pragma unroll 1
  for (int t = 0; t < 26; t += 2) {
    BARRIER_RAW(); p2_stage<NM>(F0, F1, la + ca, lw + cw, accB); adv();
    BARRIER_RAW(); p2_stage<NM>(F1, F0, la + ca, lw + cw, accB); adv();
  }
  BARRIER_RAW(); p2_stage<NM>(F0, F1, la + ca, lw + cw, accB); adv();
  ST(8);
  BARRIER_RAW(); p2_stage<NM>(F1, F0, la + ca, lw + cw, accA); adv();
#pragma unroll 1
  for (int t = 28; t < NST - 1; t += 2) {
    BARRIER_RAW(); p2_stage<NM>(F0, F1, la + ca, lw + cw, accA); adv();
    BARRIER_RAW(); p2_stage<NM>(F1, F0, la + ca, lw + cw, accA); adv();
  }
#pragma unroll
  for (int k = 0; k < 2; ++k)
#pragma unroll
    for (int m = 0; m < NM; ++m) accA[m] = MFMA32(cat(F0.lo[k], F0.hi[k]), F0.a[k][m], accA[m]);
  ST(9);
  BARRIER();
  f32x4 keep[NM][2];
#pragma unroll
  for (int m = 0; m < NM; ++m) {
    const float hv = 1.f - gv[m];
#pragma unroll
    for (int a = 0; a < 2; ++a) {
      f32x4 lo, hi;
#pragma unroll
      for (int e = 0; e < 4; ++e) {
        lo[e] = gv[m] * accA[m][4 * a + e] + hv * accB[m][4 * a + e];
        hi[e] = gv[m] * accA[m][8 + 4 * a + e] + hv * accB[m][8 + 4 * a + e];
      }
      keep[m][a] = kh ? hi : lo;
      *(LAS f32x4*)(size_t)(smb + RED_OFF + ((w * NM + m) * 2 + a) * 1024 + lane * 16) = kh ? lo : hi;
    }
  }
  BARRIER();
  char* orow = outp + (size_t)(lane & 31) * 1280 + nh * 64 + kh * 32 + h * 16;
#pragma unroll
  for (int m = 0; m < NM; ++m) {
    u32x2 v[2];
#pragma unroll
    for (int a = 0; a < 2; ++a) {
      const f32x4 o = keep[m][a] + *(LAS f32x4*)(size_t)(smb + RED_OFF + (((w ^ 2) * NM + m) * 2 + a) * 1024 + lane * 16);
      v[a] = u32x2{pack_bf16(o[0], o[1]), pack_bf16(o[2], o[3])};
    }
    const auto r0 = __builtin_amdgcn_permlane32_swap(v[0][0], v[1][0], false, false);
    const auto r1 = __builtin_amdgcn_permlane32_swap(v[0][1], v[1][1], false, false);
    *(u32x4*)(orow + m * (32 * 1280)) = u32x4{r0[0], r1[0], r0[1], r1[1]};
  }
}

__global__ __launch_bounds__(NTHREADS, 2) void fused_kernel(const u16* __restrict__ x, const u16* __restrict__ wqkv, const u16* __restrict__ wo,
                                                            const u16* __restrict__ w1, const u16* __restrict__ b1, const u16* __restrict__ w2,
                                                            const u16* __restrict__ gw, char* __restrict__ wsp, u16* __restrict__ out) {
  extern __shared__ __attribute__((aligned(16))) char smem[];
  const unsigned smb = (unsigned)(size_t)(LAS char*)smem;
  TSDECL; ST(0);
  const int lane = threadIdx.x & 63;
  const int w = __builtin_amdgcn_readfirstlane(threadIdx.x >> 6);
  int b = blockIdx.x;
  const int is_mlp = (b >> 3) & 1;
  const int rt = ((b >> 1) & 3) * 4 + ((b >> 4) & 3);
  const int cg = (b & 1) * 4 + (b >> 6);
  const int row0 = rt * 128;
  const int ps = cg < 7 ? 7 * cg : 48, np = cg < 6 ? 7 : 6;
  int k0 = ((5 * (rt & 3)) >> 1) + ((5 * (is_mlp * 4 + (cg & 3))) >> 2);
  k0 = ROT1 == 2 ? (k0 >= 9 ? k0 - 8 : k0 + 1) : (ROT1 ? (k0 >= 10 ? k0 - 10 : k0) : 0);
  const char* wmat = is_mlp ? (const char*)w1 : (const char*)wqkv;
  const unsigned vwme = p1_vw(w, lane, is_mlp, cg, ps, np);
  {
    const int kf = ROT1 == 2 ? 0 : k0;
    const unsigned RB = is_mlp ? 3456u : 3840u;
    const char* xs = (const char*)x + kf * 128;
    const char* wsrc = wmat + kf * (64 * RB);
    const unsigned d = smb + w * 1024;
    const unsigned vx = (row0 + 8 * w + (lane >> 3)) * 1280 + (((lane & 7) ^ (4 * (w & 1) + (lane >> 4))) * 16);
    glds16(xs, vx, d + 2048);
    glds16(wsrc, vwme, d + W_OFF);
    glds16(xs + 64 * 1280, vx, d + 2048 + 8192);
    glds16(wsrc + 16 * RB, vwme, d + W_OFF + 8192);
    glds16(wsrc + 32 * RB, vwme, d + W_OFF + 16384);
    glds16(wsrc + 48 * RB, vwme, d + W_OFF + 24576);
    int tokh = row0 - 16 + 2 * w + (lane >> 5); tokh = tokh < 0 ? 0 : tokh;
    const unsigned vh = tokh * 1280 + (((((lane & 31) >> 2) ^ w) * 16) | ((lane & 3) * 4));
    asm volatile("s_mov_b32 m0, %2\n\ts_nop 0\n\tglobal_load_lds_dword %0, %1" : : "v"(vh), "s"(xs), "s"(smb + w * 256) : "memory");
  }
  if (ROT1 != 2) k0 = p1_next(k0);
  u64 ep = 0;
  if (w >= 4) {
    const Side sd = {wsp, (const char*)w2, (const char*)wo, b, rt, cg, ps, np};
    {
      const char* pc;
      asm volatile("s_getpc_b64 %0" : "=s"(pc));
      const int chunk = ((b >> 3) * 4 + w) % CODE_CHUNKS;
      asm volatile("s_mov_b32 m0, %2\n\ts_mov_b32 exec_hi, 0\n\tglobal_load_lds_dword %0, %1\n\ts_mov_b32 exec_hi, -1"
                   : : "v"((unsigned)(chunk * 4096 + lane * 128)), "s"(pc), "s"(smb + 3 * K1_STAGE + w * 256) : "memory");
    }
    const unsigned vw0 = p1_vw(w - 4, lane, is_mlp, cg, ps, np);
    if (is_mlp) p1_loader<0>(smb, w - 4, lane, (const char*)x, wmat, row0, vw0, vwme, k0, sd, ep TSPASS);
    else p1_loader<1>(smb, w - 4, lane, (const char*)x, wmat, row0, vw0, vwme, k0, sd, ep TSPASS);
  }
  asm volatile("" : "+s"(b));
  const int xcd = b & 7, idx = b >> 3;
  int ct, m0;
  {
    const int j = idx >> 1, jj = j - 6;
    const int ctm = j < 12 ? j >> 2 : 3 + ((j - 12) >> 1), m0m = 3 * (j < 12 ? j & 3 : j & 1);
    const int ctq = j < 6 ? j >> 1 : 3 + (jj >= 5), m0q = j < 6 ? 12 + 2 * (j & 1) : 6 + 2 * (jj >= 5 ? jj - 5 : jj);
    ct = is_mlp ? ctm : ctq; m0 = is_mlp ? m0m : m0q;
  }
  int s0 = ROT2 ? ((m0 * 27) >> 4) + ct * 5 : 0;
  s0 = s0 >= 27 ? s0 - 27 : s0;
  ct += (xcd & 1) * 5; m0 += (xcd >> 1) * 16;
  const int nm = is_mlp ? 3 : 2;
  const rsrc_t ws = __builtin_amdgcn_make_buffer_rsrc(wsp, 0, WS_BYTES, 0x00020000);
  GAS u64* flags = (GAS u64*)(wsp + FLAG_OFF);
  GAS u64* epoch = (GAS u64*)(wsp + EPOCH_OFF);
  GAS float* gate = (GAS float*)(wsp + GATE_OFF);
  WLoad wl = {0u, 0u, nullptr, 0};
  u64 tag = 0;
  if (w >= 4) {
    wl = p2_w_init(w - 4, lane, ct, s0, w2);
    const unsigned wd = smb + (is_mlp ? Ring<3>::W_RING : Ring<2>::W_RING) + (w - 4) * 2048;
    p2_w_pre(wl, wd, 0, WP0);
    BARRIER_RAW();
    p2_w_pre(wl, wd, WP0, is_mlp ? Ring<3>::WP : Ring<2>::WP);
    asm volatile("" : "+v"(ep));
    tag = ep * 0x9E3779B97F4A7C15ull + 0x632BE59BD9B4E019ull;
    if (w >= 6) {
      if (is_mlp) {
        const int row = row0 + cg * 16 + (w - 6) * 8 + (lane >> 3);
        const char* xr = (const char*)x + row * 1280 + (lane & 7) * 16;
        const char* gr = (const char*)gw + (lane & 7) * 16;
        float s = 0.f;
#pragma unroll
        for (int t = 0; t < 10; ++t) {
          u32x4 a = *(const u32x4*)(xr + t * 128), c = *(const u32x4*)(gr + t * 128);
#pragma unroll
          for (int e = 0; e < 4; ++e) {
            const unsigned ua = a[e], uc = c[e];
            s = __builtin_amdgcn_fdot2_f32_bf16(__builtin_bit_cast(bf16x2_t, ua), __builtin_bit_cast(bf16x2_t, uc), s, false);
          }
        }
        s += __shfl_xor(s, 1); s += __shfl_xor(s, 2); s += __shfl_xor(s, 4);
        if ((lane & 7) == 0) __hip_atomic_store(gate + row, 1.f / (1.f + __expf(-s)), RLX_AGENT);
      }
    }
    ST(1); ST(2);
  } else if (is_mlp) {
    const int f0 = ps * 32;
    if (np == 7) p1_mlp<7>(smb, w, lane, f0, rt, b1, ws TSPASS);
    else p1_mlp<6>(smb, w, lane, f0, rt, b1, ws TSPASS);
  } else {
    if (w < 2) p1_qkv<3>(smb, w, lane, cg & 1 TSPASS);
    else p1_qkv<2>(smb, w, lane, cg & 1 TSPASS);
  }
  if (!is_mlp) {
    BARRIER();
    ST(3);
    p1_attn(smb, w, lane, rt, cg, ws);
  } else { ST(3); }
  ST(4);
  GAS u64* myflag = flags + rt * 16 + is_mlp * 8 + cg;
  if (is_mlp) WAIT_VM(0);
  BARRIER();
  ST(5);
  if (is_mlp && w == 6 && lane == 0) __hip_atomic_store(myflag, tag, RLX_AGENT);
  ST(6);
  if (w >= 4) {
    const int rta = m0 >> 2, rtb = (m0 + nm - 1) >> 2;
    GAS u64* fq = flags + ((lane & 8) ? rtb : rta) * 16 + (lane & 7);
    if (is_mlp) p2_loader<3>(smb, w - 4, lane, m0, s0, wl, wsp, wo, fq + 8, fq, tag, myflag);
    else p2_loader<2>(smb, w - 4, lane, m0, s0, wl, wsp, wo, fq + 8, fq, tag, myflag);
    ST(7); ST(8); ST(9);
    BARRIER_RAW();
    BARRIER_RAW();
  } else if (is_mlp) p2_compute<3>(smb, w, lane, m0, gate, (char*)out + (size_t)m0 * 32 * 1280 + ct * 128 TSPASS);
  else p2_compute<2>(smb, w, lane, m0, gate, (char*)out + (size_t)m0 * 32 * 1280 + ct * 128 TSPASS);
  if (b == 254 && w == 7) {
    for (int spin = 0; spin < SPIN_CAP; ++spin) {
      bool ok = true;
#pragma unroll
      for (int k = 0; k < 4; ++k) ok &= __hip_atomic_load(flags + k * 64 + lane, RLX_AGENT) == tag;
      if (__all(ok)) break;
      __builtin_amdgcn_s_sleep(8);
    }
    if (lane == 0) __hip_atomic_store(epoch, tag, RLX_AGENT);
  }
}

extern "C" void kernel_launch(void* const* d_in, const int* in_sizes, int n_in, void* d_out, int out_size, void* d_ws,
                              size_t ws_size, hipStream_t stream) {
  (void)hipFuncSetAttribute((const void*)fused_kernel, hipFuncAttributeMaxDynamicSharedMemorySize, LDS_BYTES);
  fused_kernel<<<256, NTHREADS, LDS_BYTES, stream>>>((const u16*)d_in[0], (const u16*)d_in[1], (const u16*)d_in[2], (const u16*)d_in[4],
                                                     (const u16*)d_in[5], (const u16*)d_in[6], (const u16*)d_in[7], (char*)d_ws, (u16*)d_out);
}
```

```cpp
#include <hip/hip_runtime.h>
#include <stdint.h>
#include <type_traits>

typedef unsigned short u16;
typedef unsigned long long u64;
typedef short bf16x8 __attribute__((ext_vector_type(8)));
typedef short s16x4 __attribute__((ext_vector_type(4)));
typedef float f32x4 __attribute__((ext_vector_type(4)));
typedef float f32x16 __attribute__((ext_vector_type(16)));
typedef float f32x2 __attribute__((ext_vector_type(2)));
typedef unsigned u32x4 __attribute__((ext_vector_type(4)));
typedef unsigned u32x2 __attribute__((ext_vector_type(2)));
typedef unsigned u32x16 __attribute__((ext_vector_type(16)));
typedef __bf16 bf16x2_t __attribute__((ext_vector_type(2)));
#define LAS __attribute__((address_space(3)))
#define GAS __attribute__((address_space(1)))
#define DI __device__ __forceinline__

#define MFMA16(a, b, c) __builtin_amdgcn_mfma_f32_16x16x32_bf16((a), (b), (c), 0, 0, 0)
#define MFMA32(a, b, c) __builtin_amdgcn_mfma_f32_32x32x16_bf16((a), (b), (c), 0, 0, 0)
#define WAIT_VM(n) asm volatile("s_waitcnt vmcnt(" #n ")" ::: "memory")
#define WAIT_VMI(n) asm volatile("s_waitcnt vmcnt(%0)" ::"i"(n) : "memory")
#define BARRIER() do { asm volatile("s_waitcnt lgkmcnt(0)" ::: "memory"); __builtin_amdgcn_s_barrier(); asm volatile("" ::: "memory"); } while (0)
#define BARRIER_RAW() do { asm volatile("" ::: "memory"); __builtin_amdgcn_s_barrier(); asm volatile("" ::: "memory"); } while (0)
#define SCHED() __builtin_amdgcn_sched_barrier(0)
#define RLX_AGENT __ATOMIC_RELAXED, __HIP_MEMORY_SCOPE_AGENT

constexpr int NTHREADS = 512;
constexpr int NKAP = 148;
constexpr int W_OFF = 18432;
constexpr int K1_STAGE = 51200;
constexpr unsigned MID_BYTES = NKAP * 64 * 1024;
constexpr unsigned GATE_OFF = MID_BYTES, FLAG_OFF = GATE_OFF + 8192, EPOCH_OFF = FLAG_OFF + 2 * 16 * 8 * 128, WS_BYTES = EPOCH_OFF + 128;
constexpr int LDS_BYTES = 163840;
constexpr int W_SLOT = 8192;
constexpr int WPQ = 6;
constexpr int WP0 = 3;
template <int NM> struct Ring {
  static constexpr int D = NM == 3 ? 8 : 10;
  static constexpr int A_SLOT = NM * 4096;
  static constexpr int W_RING = D * A_SLOT;
  static constexpr int WP = NM == 3 ? D : WPQ;
};
constexpr int NST = NKAP / 4;
constexpr int NREP = 8;
constexpr unsigned KS = 65536, MS = 1024;
constexpr int SPIN_CAP = 1 << 20;
constexpr int PFN = 1;
constexpr int CODE_CHUNKS = 10;

DI unsigned pack_bf16(float lo, float hi) {
  f32x2 f = {lo, hi};
  return __builtin_bit_cast(unsigned, __builtin_convertvector(f, bf16x2_t));
}
DI float bf_lo(unsigned u) { return __uint_as_float(u << 16); }
DI float bf_hi(unsigned u) { return __uint_as_float(u & 0xffff0000u); }
DI void glds16(const char* sbase, unsigned voff, unsigned dst) {
  asm volatile("s_mov_b32 m0, %2\n\ts_nop 0\n\tglobal_load_lds_dwordx4 %0, %1" : : "v"(voff), "s"(sbase), "s"(dst) : "memory");
}
DI void glds16_sc1(const char* sbase, unsigned voff, unsigned dst) {
  asm volatile("s_mov_b32 m0, %2\n\ts_nop 0\n\tglobal_load_lds_dwordx4 %0, %1 sc1" : : "v"(voff), "s"(sbase), "s"(dst) : "memory");
}
DI void glds16_lo(const char* sbase, unsigned voff, unsigned dst) {
  asm volatile("s_mov_b32 m0, %2\n\ts_mov_b32 exec_hi, 0\n\tglobal_load_lds_dwordx4 %0, %1\n\ts_mov_b32 exec_hi, -1" : : "v"(voff), "s"(sbase), "s"(dst) : "memory");
}
DI bf16x8 lds_b128(unsigned a) { return *(LAS bf16x8*)(size_t)a; }
DI s16x4 lds_tr(unsigned a) { return __builtin_amdgcn_ds_read_tr16_b64_v4i16((LAS s16x4*)(size_t)a); }
DI u32x2 lds_b64(unsigned a) { return *(LAS u32x2*)(size_t)a; }
DI void lds_w64(unsigned a, u32x2 v) { *(LAS u32x2*)(size_t)a = v; }
DI bf16x8 cat(s16x4 lo, s16x4 hi) { return __builtin_shufflevector(lo, hi, 0, 1, 2, 3, 4, 5, 6, 7); }
DI bf16x8 mk8(u32x2 a, u32x2 b) {
  u32x4 t = {a[0], a[1], b[0], b[1]};
  return __builtin_bit_cast(bf16x8, t);
}
typedef __amdgpu_buffer_rsrc_t rsrc_t;
DI void st128_sc1(rsrc_t r, unsigned off, u32x4 v) { __builtin_amdgcn_raw_buffer_store_b128(v, r, off, 0, 16); }
DI void st64_sc1(rsrc_t r, unsigned off, u32x2 v) { __builtin_amdgcn_raw_buffer_store_b64(v, r, off, 0, 16); }

DI void mish16(const f32x16& v, u32x4& o0, u32x4& o1) {
  f32x2 h[8], z[8], e[8], p[8], d[8], r[8];
  const f32x2 c1 = {1.44269504088896f / 160.f, 1.44269504088896f / 160.f}, c0 = {129.f / 160.f, 129.f / 160.f};
#pragma unroll
  for (int k = 0; k < 8; ++k) { h[k] = f32x2{v[2 * k], v[2 * k + 1]}; asm("v_pk_fma_f32 %0, %1, %2, %3 clamp" : "=v"(z[k]) : "v"(h[k]), "v"(c1), "v"(c0)); }
#pragma unroll
  for (int k = 0; k < 8; ++k) z[k] = z[k] * 160.f - 129.f;
  SCHED();
#pragma unroll
  for (int k = 0; k < 8; ++k) e[k] = f32x2{__builtin_amdgcn_exp2f(z[k][0]), __builtin_amdgcn_exp2f(z[k][1])};
  SCHED();
#pragma unroll
  for (int k = 0; k < 8; ++k) p[k] = e[k] + 2.f;
#pragma unroll
  for (int k = 0; k < 8; ++k) d[k] = e[k] * p[k] + 2.f;
#pragma unroll
  for (int k = 0; k < 8; ++k) p[k] = e[k] * p[k];
  SCHED();
#pragma unroll
  for (int k = 0; k < 8; ++k) r[k] = f32x2{__builtin_amdgcn_rcpf(d[k][0]), __builtin_amdgcn_rcpf(d[k][1])};
  SCHED();
#pragma unroll
  for (int k = 0; k < 8; ++k) p[k] = p[k] * r[k];
#pragma unroll
  for (int k = 0; k < 8; ++k) h[k] = h[k] * p[k];
#pragma unroll
  for (int k = 0; k < 4; ++k) { o0[k] = pack_bf16(h[k][0], h[k][1]); o1[k] = pack_bf16(h[4 + k][0], h[4 + k][1]); }
  SCHED();
}
DI float xg_max(float v) {
  unsigned u = __float_as_uint(v);
  auto r = __builtin_amdgcn_permlane32_swap(u, u, false, false);
  v = fmaxf(__uint_as_float(r[0]), __uint_as_float(r[1]));
  u = __float_as_uint(v);
  auto r2 = __builtin_amdgcn_permlane16_swap(u, u, false, false);
  return fmaxf(__uint_as_float(r2[0]), __uint_as_float(r2[1]));
}
DI float xg_sum(float v) {
  unsigned u = __float_as_uint(v);
  auto r = __builtin_amdgcn_permlane32_swap(u, u, false, false);
  v = __uint_as_float(r[0]) + __uint_as_float(r[1]);
  u = __float_as_uint(v);
  auto r2 = __builtin_amdgcn_permlane16_swap(u, u, false, false);
  return __uint_as_float(r2[0]) + __uint_as_float(r2[1]);
}

template <int NM>
constexpr int p2_issued(int T) {
  const int n = T < 0 ? 4 : (T == 0 ? 6 : 2 * T + Ring<NM>::D);
  return n > NST ? NST : n;
}
template <int NM>
constexpr int p2_allowed(int T) {
  int n = 0;
  for (int st = 2 * T + 4; st < p2_issued<NM>(T); ++st) n += st < Ring<NM>::WP ? NM : NM + 2;
  return n;
}
template <int I, int N, class F>
DI void static_for(F&& f) {
  if constexpr (I < N) { f(std::integral_constant<int, I>{}); static_for<I + 1, N>(f); }
}
struct WLoad { unsigned voffn, voffe; const char* src; int st; };
DI WLoad p2_w_init(int l, int lane, int ct, int s0, const u16* __restrict__ w2) {
  const int r8 = lane >> 3;
  const unsigned colb = ct * 128 + (((lane & 7) ^ (((r8 >> 1) & 1) << 2)) * 16);
  WLoad s;
  s.voffn = r8 * 1280 + colb;
  s.voffe = (20 * (r8 >> 2) + (r8 & 3)) * 1280 + colb;
  s.src = (const char*)w2 + (16 * l) * 1280;
  s.st = s0;
  return s;
}
DI void p2_w_pre(WLoad& s, unsigned d, int t0, int t1) {
#pragma unroll 1
  for (int t = t0; t < t1; ++t) {
    const char* p = s.src + s.st * (64 * 1280);
    glds16(p, s.voffn, d + t * W_SLOT);
    glds16(p + 8 * 1280, s.voffn, d + t * W_SLOT + 1024);
    s.st = s.st == 26 ? 0 : s.st + 1;
  }
}
DI u64 flag_load_async(GAS u64* p) {
  u64 v;
  asm volatile("global_load_dwordx2 %0, %1, off sc1" : "=v"(v) : "v"(p) : "memory");
  return v;
}
constexpr int PSLEEP = 2;
DI void spin_flags(GAS u64* fp, u64 tag) {
#pragma unroll 1
  for (int spin = 0; spin < SPIN_CAP; ++spin) {
    if (__all(__hip_atomic_load(fp, RLX_AGENT) == tag)) break;
    __builtin_amdgcn_s_sleep(PSLEEP);
  }
  asm volatile("" ::: "memory");
}
DI void flag_check(u64& v, GAS u64* fp, u64 tag) {
  asm volatile("s_waitcnt vmcnt(0)" : "+v"(v) :: "memory");
  if (!__all(v == tag)) spin_flags(fp, tag);
}
template <int NM>
DI void p2_loader(unsigned smb, int l, int lane, int m0, int sa, WLoad& ws, const char* __restrict__ mid, const u16* __restrict__ wo,
                  GAS u64* fpq, u64 tag, GAS u64* myflag) {
  constexpr int D = Ring<NM>::D, A_SLOT = Ring<NM>::A_SLOT, DS = D / 2, NW = (DS - 2) * 2 * (NM + 2);
  const unsigned lane16 = lane * 16;
  const char* src = mid + (40 + l) * KS + m0 * MS;
  const unsigned da = smb + l * (NM * 1024), dw = smb + Ring<NM>::W_RING + l * 2048;
  unsigned ps = 0;
  auto issue_a = [&]() {
    const char* p = src + sa * (4 * KS);
#pragma unroll
    for (int m = 0; m < NM; ++m) glds16_sc1(p + m * MS, lane16, da + ps * A_SLOT + m * 1024);
    sa = sa == 26 ? 0 : sa + 1;
    ps = (ps == D - 1) ? 0 : ps + 1;
  };
  auto issue_act = [&]() {
    const char* p = ws.src + sa * (64 * 1280);
    glds16(p, ws.voffn, dw + ps * W_SLOT);
    glds16(p + 8 * 1280, ws.voffn, dw + ps * W_SLOT + 1024);
    issue_a();
  };
  int u = l;
  const char* won = (const char*)wo;
  auto issue_att = [&]() {
    const int r0 = u < 4 ? 40 * (u >> 1) + 8 * (u & 1) : 16, dr = u < 4 ? 20 : 40;
    const unsigned vo = u < 4 ? ws.voffn : ws.voffe;
    glds16(won + r0 * 1280, vo, dw + ps * W_SLOT);
    glds16(won + (r0 + dr) * 1280, vo, dw + ps * W_SLOT + 1024);
    won += u >= 1 ? 80 * 1280 : 0;
    u = u >= 1 ? u - 1 : 4;
    issue_a();
  };
  static_assert(Ring<NM>::WP >= 6 && Ring<NM>::WP <= D + 2, "");
#pragma unroll 1
  for (int t = 0; t < 4; ++t) issue_a();
  WAIT_VMI((p2_allowed<NM>(-1)));
  BARRIER_RAW();
  if (NM == 2 && l == 2 && lane < NREP) {
    asm volatile("global_store_dwordx2 %0, %1, off sc1" : : "v"(myflag), "v"(tag) : "memory");
  }
  issue_a(); issue_a();
  u64 fq = flag_load_async(fpq + 2048);
  WAIT_VMI(p2_allowed<NM>(0) + 1); BARRIER_RAW();
  flag_check(fq, fpq + 2048, tag);
#pragma unroll 1
  for (int t = 6; t < Ring<NM>::WP; ++t) issue_a();
#pragma unroll 1
  for (int t = Ring<NM>::WP; t < D + 2; ++t) issue_act();
  WAIT_VMI((p2_allowed<NM>(1))); BARRIER_RAW();
  issue_act(); issue_act();
  static_assert(p2_allowed<NM>(2) == NW, "steady state from B_2 on");
#pragma unroll 1
  for (int T = 2; T + DS <= 11; ++T) { WAIT_VMI(NW); BARRIER_RAW(); issue_act(); issue_act(); }
  fq = flag_load_async(fpq);
  WAIT_VMI(NW); BARRIER_RAW(); issue_act(); issue_act();
  WAIT_VMI(NW); BARRIER_RAW(); issue_act();
  asm volatile("s_waitcnt vmcnt(%1)" : "+v"(fq) : "i"(3 * (NM + 2)) : "memory");
  if (!__all(fq == tag)) spin_flags(fpq, tag);
  src = mid + l * KS + m0 * MS; sa = 0;
  issue_att();
#pragma unroll 1
  for (int j = 0; j < 4; ++j) { WAIT_VMI(NW); BARRIER_RAW(); issue_att(); issue_att(); }
  WAIT_VMI(NW); BARRIER_RAW(); issue_att();
  static_for<19 - DS, 18>([&](auto tc) { WAIT_VMI((p2_allowed<NM>(tc.value))); BARRIER_RAW(); });
}

DI void qkv_slot(int sg, int odd, int& P, int& beta) {
  if (sg < 12) { P = sg >> 2; beta = (sg & 3) + odd; }
  else { P = sg < 15 ? sg - 12 : 2; beta = odd ? 0 : 4; }
}
DI unsigned p1_vw(int v, int lane, int is_mlp, int cg, int ps, int np) {
  const int hi = lane >> 5;
  const int c = (lane & 31) ^ ((((2 * v + hi) & 3) << 2) | ((v >> 2) << 1));
  const int sg = c >> 1, odd = cg & 1;
  const int P = sg < 12 ? sg >> 2 : (sg < 15 ? sg - 12 : 2);
  const int beta = sg < 12 ? (sg & 3) + odd : (odd ? 0 : 4);
  const unsigned colq = (P == 2 ? 0 : (P + 1) * 1280) + cg * 160 + beta * 32 + (c & 1) * 16;
  const unsigned colm = ps * 64 + (c < 4 * np ? c : 4 * np - 1) * 16;
  return (2 * v + hi) * (is_mlp ? 3456u : 3840u) + (is_mlp ? colm : colq);
}
constexpr int PRE1 = 5;
DI int p1_next(int kk) { return kk == 9 ? 1 : kk + 1; }
template <int QKV>
DI void p1_loader(unsigned smb, int L, int lane, const char* __restrict__ x, const char* __restrict__ wmat, int row0, unsigned vw0, unsigned vw1, int kk,
                  const char* wsp, u64& ep, const char* w2, const char* wo, unsigned tv, u32x4& td) {
  constexpr unsigned RB = QKV ? 3840 : 3456;
  constexpr int NL = QKV ? 13 : 12;
  const unsigned vx = (row0 + 8 * L + (lane >> 3)) * 1280 + (((lane & 7) ^ (4 * (L & 1) + (lane >> 4))) * 16);
  const int hr = 4 * L + ((lane >> 3) & 3);
  int tokh = row0 - 16 + hr; tokh = tokh < 0 ? 0 : tokh;
  const unsigned vh = tokh * 1280 + (((lane & 7) ^ ((hr >> 1) & 7)) * 16);
  auto piece = [&](int p, unsigned buf) {
    const unsigned d = smb + buf + L * 1024;
    const char* xs = x + kk * 128;
    const char* wsrc = wmat + kk * (64 * RB);
    if (p < 8) { if (p & 1) glds16(xs + (p >> 1) * (32 * 1280), vx, d + 2048 + (p >> 1) * 4096);
                 else glds16(wsrc + (p >> 2) * (16 * RB), (p & 2) ? vw1 : vw0, d + W_OFF + (p >> 1) * 4096); }
    else if (p < 12) glds16(wsrc + ((p - 4) >> 1) * (16 * RB), (p & 1) ? vw1 : vw0, d + W_OFF + (p - 4) * 4096);
    else glds16_lo(xs, vh, smb + buf + L * 512);
  };
  auto issue = [&](unsigned buf) {
#pragma unroll
    for (int p = 0; p < NL; ++p) piece(p, buf);
    kk = p1_next(kk);
  };
#pragma unroll
  for (int p = 0; p < PRE1; ++p) piece(p, K1_STAGE);
  WAIT_VMI(PRE1 + PFN);
  BARRIER_RAW();
  asm volatile("global_load_dwordx2 %0, %1, %2 sc1" : "=v"(ep) : "v"(EPOCH_OFF), "s"(wsp) : "memory");
#pragma unroll
  for (int p = PRE1; p < NL; ++p) piece(p, K1_STAGE);
  kk = p1_next(kk);
  issue(2 * K1_STAGE);
  unsigned nb = 0;
#pragma unroll 1
  for (int t = 0; t < 7; ++t) {
    WAIT_VMI(NL);
    BARRIER_RAW();
    issue(nb);
    nb = (nb == 2 * K1_STAGE) ? 0 : nb + K1_STAGE;
  }
  asm volatile("global_load_dword %0, %3, %4\n\tglobal_load_dword %1, %3, %5\n\tglobal_load_dword %2, %3, %6"
               : "=&v"(td[0]), "=&v"(td[1]), "=&v"(td[2]) : "v"(tv), "s"(w2 + 64 * 1280), "s"(w2 + 704 * 1280), "s"(wo) : "memory");
  WAIT_VMI(NL + 3);
  BARRIER_RAW();
  WAIT_VMI(3);
  BARRIER_RAW();
}

struct Frag { bf16x8 x[2]; s16x4 lo[4], hi[4]; };
struct HFrag { bf16x8 x; s16x4 lo[3], hi[3]; };

template <int NMM>
DI void p1_read(Frag& F, int idx, unsigned ax0, unsigned ax1, const unsigned (&aw)[4], int s) {
  if (idx == 0) F.x[0] = lds_b128(ax0);
  else if (idx == 3) F.x[1] = lds_b128(ax1);
  else if (idx < (NMM > 6 ? 10 : 8)) {
    const int k = idx < 3 ? idx - 1 : idx - 2;
    if ((k & 1) == 0) F.lo[k >> 1] = lds_tr(aw[k >> 1] + W_OFF + s * 8192);
    else F.hi[k >> 1] = lds_tr(aw[k >> 1] + W_OFF + s * 8192 + 2048);
  }
}

template <int NMM, int NH>
DI void p1_compute(f32x16 (&acc)[2][4], f32x4 (&ah)[3], unsigned smb, unsigned lx0, unsigned lx1, const unsigned (&lw)[4],
                   unsigned lxh, const unsigned (&lh)[3]) {
  Frag F[2];
  HFrag H;
  unsigned aw[4];
#pragma unroll
  for (int n = 0; n < 4; ++n) aw[n] = smb + lw[n];
  WAIT_VMI(NH ? 0 : (NMM > 6 ? 4 : 3));
  BARRIER_RAW();
#pragma unroll
  for (int i = 0; i < 10; ++i) p1_read<NMM>(F[0], i, smb + lx0, smb + lx1, aw, 0);
  unsigned cur = smb;
#pragma unroll 1
  for (int t = 0; t < 10; ++t) {
    const unsigned nxt = (cur == smb + 2 * K1_STAGE) ? smb : cur + K1_STAGE;
#pragma unroll
    for (int s = 0; s < 4; ++s) {
      const Frag& C = F[s & 1];
      Frag& N = F[(s + 1) & 1];
      const unsigned nb = s == 3 ? nxt : cur;
      const int ns = (s + 1) & 3;
      const unsigned ax0 = nb + (lx0 ^ (32 * ns)), ax1 = nb + (lx1 ^ (32 * ns));
#pragma unroll
      for (int mm = 0; mm < NMM; ++mm) {
        const int n = mm >> 1, m = mm & 1;
        acc[m][n] = MFMA32(cat(C.lo[n], C.hi[n]), C.x[m], acc[m][n]);
        if (s == 3 && mm == 0) {
          SCHED();
          BARRIER();
#pragma unroll
          for (int q = 0; q < 4; ++q) aw[q] = nxt + lw[q];
        }
        p1_read<NMM>(N, 2 * mm, ax0, ax1, aw, ns); p1_read<NMM>(N, 2 * mm + 1, ax0, ax1, aw, ns);
        if (NH) {
          if (mm == 4 && (s == 1 || s == 2)) {
#pragma unroll
            for (int j = 0; j < NH; ++j) ah[j] = MFMA16(cat(H.lo[j], H.hi[j]), H.x, ah[j]);
          }
          if (mm >= 5 && s < 2) {
            if (mm == 5) H.x = lds_b128(cur + (lxh ^ (64 * s)));
            if (mm - 5 < NH) { H.lo[mm - 5] = lds_tr(cur + lh[mm - 5] + W_OFF + s * 16384); H.hi[mm - 5] = lds_tr(cur + lh[mm - 5] + W_OFF + s * 16384 + 2048); }
          }
        }
        SCHED();
      }
    }
    cur = nxt;
  }
}

DI unsigned p1_lx(int m32, int lane) {
  const int r = lane & 31;
  return (16 + 32 * m32 + r) * 128 + ((((lane >> 5)) ^ ((r >> 1) & 7)) * 16);
}
DI unsigned p1_lw(int T, int lane) {
  const int h = lane >> 5, q = (lane >> 2) & 3, p = lane & 3, cb = (lane >> 4) & 1;
  return (8 * h + q) * 512 + ((64 * T + 32 * cb + 8 * p) ^ ((q << 6) | (h << 5)));
}

template <int NMM>
DI void p1_mlp(unsigned smb, int w, int lane, int f0, int rt, const u16* __restrict__ b1, rsrc_t ws) {
  const int h = lane >> 5;
  const int nh = w >> 1;
  const int ma = 2 * (w & 1) + nh, mb = ma ^ 1;
  const int T[4] = {3 * nh, 3 * nh + 1, 3 * nh + 2, 6};
  f32x16 acc[2][4]; f32x4 ah[3];
  unsigned lw[4];
  unsigned bv[4];
#pragma unroll
  for (int n = 0; n < 4; ++n) {
    lw[n] = p1_lw(T[n], lane);
    if (n < 3 || NMM > 6)
      asm volatile("global_load_ushort %0, %1, %2" : "=v"(bv[n]) : "v"((unsigned)((f0 + 32 * T[n] + (lane & 31)) * 2)), "s"(b1) : "memory");
    acc[0][n] = f32x16{}; acc[1][n] = f32x16{};
    asm volatile("" : "+v"(acc[0][n]), "+v"(acc[1][n]));
  }
  const unsigned lh[3] = {0u, 0u, 0u};
  p1_compute<NMM, 0>(acc, ah, smb, p1_lx(ma, lane), p1_lx(mb, lane), lw, 0u, lh);
  {
    WAIT_VM(0);
    const u32x4 one = {h ? 0u : 0x3f80u, 0u, 0u, 0u};
#pragma unroll
    for (int mm = 0; mm < NMM; ++mm) {
      const int n = mm >> 1, m = mm & 1;
      asm volatile("" : "+v"(bv[n]));
      const u32x4 bf = {h ? 0u : bv[n], 0u, 0u, 0u};
      acc[m][n] = MFMA32(__builtin_bit_cast(bf16x8, bf), __builtin_bit_cast(bf16x8, one), acc[m][n]);
    }
  }
  if constexpr (NMM > 6)
    asm volatile("s_nop 15\n\ts_nop 15" : "+v"(acc[0][0]), "+v"(acc[1][0]), "+v"(acc[0][1]), "+v"(acc[1][1]), "+v"(acc[0][2]), "+v"(acc[1][2]), "+v"(acc[0][3]));
  else
    asm volatile("s_nop 15\n\ts_nop 15" : "+v"(acc[0][0]), "+v"(acc[1][0]), "+v"(acc[0][1]), "+v"(acc[1][1]), "+v"(acc[0][2]), "+v"(acc[1][2]));
#pragma unroll
  for (int mm = 0; mm < NMM; ++mm) {
    const int n = mm >> 1, m = mm & 1;
    const unsigned dst = (40 + (f0 >> 4) + 2 * T[n]) * KS + (rt * 4 + (m ? mb : ma)) * MS + lane * 16;
    u32x4 o0, o1;
    mish16(acc[m][n], o0, o1);
    st128_sc1(ws, dst, o0); st128_sc1(ws, dst + KS, o1);
  }
}

constexpr int XMT = 7680, XDUMP = 9 * XMT;
DI unsigned xslot_off(int sg, int odd) {
  int part, beta; qkv_slot(sg, odd, part, beta);
  return part * 2560 + beta * 512;
}

template <int NH>
DI void p1_qkv(unsigned smb, int w, int lane, int odd) {
  const int h = lane >> 5, g = lane >> 4, i = lane & 15;
  const int ma = 2 * (w & 1), T0 = 4 * (w >> 1);
  f32x16 acc[2][4] = {}; f32x4 ah[3] = {};
  const unsigned lxh = i * 128 + ((g ^ (i >> 1)) * 16);
  const int q = (lane >> 2) & 3, p = lane & 3;
  const unsigned hrow = (8 * g + q) * 512, hsw = (q << 6) | ((g & 1) << 5);
  const int hs[3] = {2 * w, 2 * w + 1, 12 + (w & 1)};
  unsigned lh[3], lw[4];
#pragma unroll
  for (int j = 0; j < 3; ++j) lh[j] = hrow + ((32 * hs[j] + 8 * p) ^ hsw);
#pragma unroll
  for (int n = 0; n < 4; ++n) { lw[n] = p1_lw(T0 + n, lane); asm volatile("" : "+v"(acc[0][n]), "+v"(acc[1][n])); }
  p1_compute<8, NH>(acc, ah, smb, p1_lx(ma, lane), p1_lx(ma + 1, lane), lw, lxh, lh);
  const unsigned lb = smb + (1 + 2 * ma + (g & 1)) * XMT + h * 128 + i * 8;
#pragma unroll
  for (int n = 0; n < 4; ++n)
#pragma unroll
    for (int a2 = 0; a2 < 2; ++a2) {
      const int sg = 2 * (T0 + n) + a2;
      if (n == 3 && a2 == 1 && sg == 15) continue;
      const unsigned xo = lb + xslot_off(sg, odd);
#pragma unroll
      for (int a1 = 0; a1 < 2; ++a1)
#pragma unroll
        for (int m = 0; m < 2; ++m) {
          const int a = 2 * a2 + a1;
          lds_w64(xo + a1 * 256 + m * 2 * XMT, u32x2{pack_bf16(acc[m][n][4 * a], acc[m][n][4 * a + 1]), pack_bf16(acc[m][n][4 * a + 2], acc[m][n][4 * a + 3])});
        }
    }
#pragma unroll
  for (int j = 0; j < NH; ++j)
    lds_w64(smb + xslot_off(hs[j], odd) + lane * 8, u32x2{pack_bf16(ah[j][0], ah[j][1]), pack_bf16(ah[j][2], ah[j][3])});
}

DI void p1_attn(unsigned smb, int w, int lane, int rt, int hg, rsrc_t ws, GAS u64* fpm, u64& fm) {
  const int g = lane >> 4, i = lane & 15;
  const unsigned base = smb + w * XMT;
  const unsigned pa = base + lane * 8, pe = base + (5 * g + 4) * 128 + i * 8;
  const unsigned va = base + 2560 + (lane & 3) * 128 + (lane >> 2) * 8, ve = base + 2560 + (5 * (lane & 3) + 4) * 128 + (lane >> 2) * 8;
  u32x2 q[5], kp[5], kc[5], vp[5], vc[5];
#pragma unroll
  for (int t = 0; t < 5; ++t) {
    const unsigned a = t < 4 ? pa + t * 640 : pe;
    q[t] = lds_b64(a + XMT + 5120);
    kp[t] = lds_b64(a); kc[t] = lds_b64(a + XMT);
  }
#pragma unroll
  for (int t = 0; t < 5; ++t) {
    const unsigned a = t < 4 ? va + t * 640 : ve;
    vp[t] = __builtin_bit_cast(u32x2, lds_tr(a)); vc[t] = __builtin_bit_cast(u32x2, lds_tr(a + XMT));
  }
  const int par = i & 1;
  const bool c0 = (4 * g + par) <= i, c1 = (4 * g + par + 2) <= i;
  const bool first = (rt == 0) && (w == 0);
  const unsigned hm = par ? 0xffff0000u : 0x0000ffffu;
  const unsigned m_p0 = c0 ? 0u : hm, m_c0 = c0 ? hm : 0u, m_p1 = c1 ? 0u : hm, m_c1 = c1 ? hm : 0u;
  const float cs = 0.22360679774997896f * 1.44269504088896f;
  const f32x4 z4 = {0.f, 0.f, 0.f, 0.f};
  f32x4 oe = z4;
  float inv_e = 0.f;
  u32x2 op[4];
#pragma unroll
  for (int j = 0; j < 4; ++j) {
    u32x2 qe = q[4]; if (g != j) qe = u32x2{0u, 0u};
    bf16x8 qop = mk8(q[j], qe);
    f32x4 sp = MFMA16(mk8(kp[j], kp[4]), qop, z4);
    f32x4 sc = MFMA16(mk8(kc[j], kc[4]), qop, z4);
    float a0 = par ? sc[1] : sc[0], a1 = par ? sc[3] : sc[2];
    float b0 = par ? sp[1] : sp[0], b1 = par ? sp[3] : sp[2];
    if (first) { b0 = -INFINITY; b1 = -INFINITY; }
    float s0 = c0 ? a0 : b0, s1 = c1 ? a1 : b1;
    float mx = xg_max(fmaxf(s0, s1));
    float nm = -mx * cs;
    float p0 = __builtin_amdgcn_exp2f(fmaf(s0, cs, nm)), p1 = __builtin_amdgcn_exp2f(fmaf(s1, cs, nm));
    unsigned w0 = pack_bf16(p0, p0), w1 = pack_bf16(p1, p1);
    float sum = xg_sum(bf_lo(w0) + bf_lo(w1));
    float inv = __builtin_amdgcn_rcpf(sum);
    u32x4 pw = {w0 & m_p0, w1 & m_p1, w0 & m_c0, w1 & m_c1};
    bf16x8 pop = __builtin_bit_cast(bf16x8, pw);
    f32x4 o = MFMA16(mk8(vp[j], vc[j]), pop, z4);
    u32x2 ep = vp[4], ec = vc[4];
    if ((i >> 2) != j) { ep = u32x2{0u, 0u}; ec = u32x2{0u, 0u}; }
    oe = MFMA16(mk8(ep, ec), pop, oe);
    if (g == j) inv_e = inv;
    op[j] = u32x2{pack_bf16(o[0] * inv, o[1] * inv), pack_bf16(o[2] * inv, o[3] * inv)};
  }
  if (w == 4) fm = flag_load_async(fpm);
  const int m32 = rt * 4 + (w >> 1);
  const unsigned lp = (32 * (g & 1) + 16 * (w & 1) + i) * 16;
  const unsigned d0 = (5 * hg + (g >> 1)) * KS + m32 * MS + lp;
  st128_sc1(ws, d0, u32x4{op[0][0], op[0][1], op[1][0], op[1][1]});
  st128_sc1(ws, d0 + 2 * KS, u32x4{op[2][0], op[2][1], op[3][0], op[3][1]});
  st64_sc1(ws, (5 * hg + 4) * KS + m32 * MS + lp + (g >> 1) * 8,
           u32x2{pack_bf16(oe[0] * inv_e, oe[1] * inv_e), pack_bf16(oe[2] * inv_e, oe[3] * inv_e)});
}

template <int NM> struct Frag2 { bf16x8 a[2][NM]; s16x4 lo[2], hi[2]; };
template <int NM>
DI void p2_stage(const Frag2<NM>& C, Frag2<NM>& N, unsigned pa, unsigned pw, f32x16 (&acc)[NM]) {
#pragma unroll
  for (int k = 0; k < 2; ++k) {
#pragma unroll
    for (int m = 0; m < NM; ++m) {
      acc[m] = MFMA32(cat(C.lo[k], C.hi[k]), C.a[k][m], acc[m]);
      if (m == 0) { N.lo[k] = lds_tr(pw + k * 2048); N.hi[k] = lds_tr(pw + k * 2048 + 1024); }
      N.a[k][m] = lds_b128(pa + (k * NM + m) * 1024);
      SCHED();
    }
  }
}
template <int NM>
DI void p2_compute(unsigned smb, int w, int lane, int m0, GAS float* gate, char* __restrict__ outp) {
  const int kh = w >> 1, nh = w & 1, h = lane >> 5;
  constexpr int D = Ring<NM>::D, A_SLOT = Ring<NM>::A_SLOT;
  const unsigned la = smb + kh * (2 * NM * 1024) + lane * 16;
  const int q = (lane >> 2) & 3;
  const unsigned lw = smb + Ring<NM>::W_RING + kh * 4096 + (4 * h + q) * 128 + ((64 * nh + 32 * ((lane >> 4) & 1) + 8 * (lane & 3)) ^ ((q >> 1) << 6));
  f32x16 accA[NM] = {}, accB[NM] = {};
  Frag2<NM> F0, F1;
#pragma unroll
  for (int m = 0; m < NM; ++m) asm volatile("" : "+v"(accA[m]), "+v"(accB[m]));
  WAIT_VM(0);
  BARRIER_RAW();
#pragma unroll
  for (int k = 0; k < 2; ++k) {
    F0.lo[k] = lds_tr(lw + k * 2048); F0.hi[k] = lds_tr(lw + k * 2048 + 1024);
#pragma unroll
    for (int m = 0; m < NM; ++m) F0.a[k][m] = lds_b128(la + (k * NM + m) * 1024);
  }
  unsigned ca = A_SLOT, cw = W_SLOT;
  auto adv = [&]() { ca = (ca == (D - 1) * A_SLOT) ? 0 : ca + A_SLOT; cw = (cw == (D - 1) * W_SLOT) ? 0 : cw + W_SLOT; };
#pragma unroll 1
  for (int T = 0; T < 13; ++T) {
    p2_stage<NM>(F0, F1, la + ca, lw + cw, accB); adv();
    BARRIER(); p2_stage<NM>(F1, F0, la + ca, lw + cw, accB); adv();
  }
  float gv[NM];
#pragma unroll
  for (int m = 0; m < NM; ++m) gv[m] = __hip_atomic_load(gate + (m0 + m) * 32 + (lane & 31), RLX_AGENT);
  p2_stage<NM>(F0, F1, la + ca, lw + cw, accB); adv();
  BARRIER(); p2_stage<NM>(F1, F0, la + ca, lw + cw, accA); adv();
#pragma unroll
  for (int m = 0; m < NM; ++m) { accB[m] *= 1.f - gv[m]; asm volatile("" : "+v"(accB[m])); }
#pragma unroll 1
  for (int T = 14; T < 18; ++T) {
    p2_stage<NM>(F0, F1, la + ca, lw + cw, accA); adv();
    BARRIER(); p2_stage<NM>(F1, F0, la + ca, lw + cw, accA); adv();
  }
#pragma unroll
  for (int k = 0; k < 2; ++k)
#pragma unroll
    for (int m = 0; m < NM; ++m) accA[m] = MFMA32(cat(F0.lo[k], F0.hi[k]), F0.a[k][m], accA[m]);
  constexpr int RED_OFF = (32 % D) * A_SLOT;
  static_assert(33 % D == 32 % D + 1, "two adjacent slots");
  f32x4 keep[NM][2];
#pragma unroll
  for (int m = 0; m < NM; ++m) {
#pragma unroll
    for (int a = 0; a < 2; ++a) {
      f32x4 lo, hi;
#pragma unroll
      for (int e = 0; e < 4; ++e) {
        lo[e] = fmaf(gv[m], accA[m][4 * a + e], accB[m][4 * a + e]);
        hi[e] = fmaf(gv[m], accA[m][8 + 4 * a + e], accB[m][8 + 4 * a + e]);
      }
      keep[m][a] = kh ? hi : lo;
      *(LAS f32x4*)(size_t)(smb + RED_OFF + ((w * NM + m) * 2 + a) * 1024 + lane * 16) = kh ? lo : hi;
    }
  }
  BARRIER();
  char* orow = outp + (size_t)(lane & 31) * 1280 + nh * 64 + kh * 32 + h * 16;
#pragma unroll
  for (int m = 0; m < NM; ++m) {
    u32x2 v[2];
#pragma unroll
    for (int a = 0; a < 2; ++a) {
      const f32x4 o = keep[m][a] + *(LAS f32x4*)(size_t)(smb + RED_OFF + (((w ^ 2) * NM + m) * 2 + a) * 1024 + lane * 16);
      v[a] = u32x2{pack_bf16(o[0], o[1]), pack_bf16(o[2], o[3])};
    }
    const auto r0 = __builtin_amdgcn_permlane32_swap(v[0][0], v[1][0], false, false);
    const auto r1 = __builtin_amdgcn_permlane32_swap(v[0][1], v[1][1], false, false);
    *(u32x4*)(orow + m * (32 * 1280)) = u32x4{r0[0], r1[0], r0[1], r1[1]};
  }
}

__global__ __launch_bounds__(NTHREADS, 2) void fused_kernel(const u16* __restrict__ x, const u16* __restrict__ wqkv, const u16* __restrict__ wo,
                                                            const u16* __restrict__ w1, const u16* __restrict__ b1, const u16* __restrict__ w2,
                                                            const u16* __restrict__ gw, char* __restrict__ wsp, u16* __restrict__ out) {
  extern __shared__ __attribute__((aligned(16))) char smem[];
  const unsigned smb = (unsigned)(size_t)(LAS char*)smem;
  const int lane = threadIdx.x & 63;
  const int w = __builtin_amdgcn_readfirstlane(threadIdx.x >> 6);
  int b = blockIdx.x;
  const int is_mlp = (b >> 3) & 1;
  const int rt = ((b >> 2) & 1) * 8 + (b >> 5);
  const int cg = (b & 3) * 2 + ((b >> 4) & 1);
  const int row0 = rt * 128;
  const int ps = cg < 7 ? 7 * cg : 48, np = cg < 6 ? 7 : 6;
  const int k0 = 1 + ((((rt & 3) + (cg & 1) + 2 * is_mlp) & 3) * 9 >> 2);
  const char* wmat = is_mlp ? (const char*)w1 : (const char*)wqkv;
  const unsigned vwme = p1_vw(w, lane, is_mlp, cg, ps, np);
  {
    const unsigned RB = is_mlp ? 3456u : 3840u;
    const char* xs = (const char*)x;
    const char* wsrc = wmat;
    const unsigned d = smb + w * 1024;
    const unsigned vx = (row0 + 8 * w + (lane >> 3)) * 1280 + (((lane & 7) ^ (4 * (w & 1) + (lane >> 4))) * 16);
    glds16(xs, vx, d + 2048);
    glds16(wsrc, vwme, d + W_OFF);
    glds16(xs + 64 * 1280, vx, d + 2048 + 8192);
    glds16(wsrc + 16 * RB, vwme, d + W_OFF + 8192);
    glds16(wsrc + 32 * RB, vwme, d + W_OFF + 16384);
    glds16(wsrc + 48 * RB, vwme, d + W_OFF + 24576);
    int tokh = row0 - 16 + 2 * w + (lane >> 5); tokh = tokh < 0 ? 0 : tokh;
    const unsigned vh = tokh * 1280 + (((((lane & 31) >> 2) ^ w) * 16) | ((lane & 3) * 4));
    asm volatile("s_mov_b32 m0, %2\n\ts_nop 0\n\tglobal_load_lds_dword %0, %1" : : "v"(vh), "s"(xs), "s"(smb + w * 256) : "memory");
  }
  u64 ep = 0;
  u32x4 td = {0u, 0u, 0u, 0u};
  if (w >= 4) {
    {
      const char* pc;
      asm volatile("s_getpc_b64 %0" : "=s"(pc));
      const int chunk = ((b >> 3) * 4 + w) % CODE_CHUNKS;
      asm volatile("s_mov_b32 m0, %2\n\ts_mov_b32 exec_hi, 0\n\tglobal_load_lds_dword %0, %1\n\ts_mov_b32 exec_hi, -1"
                   : : "v"((unsigned)(chunk * 4096 + lane * 128)), "s"(pc), "s"(smb + 3 * K1_STAGE + w * 256) : "memory");
    }
    const unsigned vw0 = p1_vw(w - 4, lane, is_mlp, cg, ps, np);
    const int tid2 = ((b >> 3) * 4 + (w - 4)) * 25 + (lane < 25 ? lane : 24), tr2 = tid2 / 5;
    const unsigned tv = tr2 * 1280 + ((b & 1) * 5 + tid2 - tr2 * 5) * 128;
    if (is_mlp) p1_loader<0>(smb, w - 4, lane, (const char*)x, wmat, row0, vw0, vwme, k0, wsp, ep, (const char*)w2, (const char*)wo, tv, td);
    else p1_loader<1>(smb, w - 4, lane, (const char*)x, wmat, row0, vw0, vwme, k0, wsp, ep, (const char*)w2, (const char*)wo, tv, td);
  }
  asm volatile("" : "+s"(b));
  const int xcd = b & 7, idx = b >> 3;
  int ct, m0;
  {
    const int j = idx >> 1, jj = j - 6;
    const int ctm = j < 12 ? j >> 2 : 3 + ((j - 12) >> 1), m0m = 3 * (j < 12 ? j & 3 : j & 1);
    const int ctq = j < 6 ? j >> 1 : 3 + (jj >= 5), m0q = j < 6 ? 12 + 2 * (j & 1) : 6 + 2 * (jj >= 5 ? jj - 5 : jj);
    ct = is_mlp ? ctm : ctq; m0 = is_mlp ? m0m : m0q;
  }
  const int s0 = 21;
  ct += (xcd & 1) * 5; m0 += (xcd >> 1) * 16;
  const int nm = is_mlp ? 3 : 2;
  const rsrc_t ws = __builtin_amdgcn_make_buffer_rsrc(wsp, 0, WS_BYTES, 0x00020000);
  GAS u64* flags = (GAS u64*)(wsp + FLAG_OFF);
  GAS u64* epoch = (GAS u64*)(wsp + EPOCH_OFF);
  GAS float* gate = (GAS float*)(wsp + GATE_OFF);
  WLoad wl = {0u, 0u, nullptr, 0};
  u64 tag = 0;
  if (w >= 4) {
    wl = p2_w_init(w - 4, lane, ct, s0, w2);
    const unsigned wd = smb + (is_mlp ? Ring<3>::W_RING : Ring<2>::W_RING) + (w - 4) * 2048;
    p2_w_pre(wl, wd, 0, WP0);
    BARRIER_RAW();
    p2_w_pre(wl, wd, WP0, is_mlp ? Ring<3>::WP : Ring<2>::WP);
    asm volatile("" : "+v"(ep));
    tag = ep * 0x9E3779B97F4A7C15ull + 0x632BE59BD9B4E019ull;
    if (w >= 6) {
      if (is_mlp) {
        const int row = row0 + cg * 16 + (w - 6) * 8 + (lane >> 3);
        const char* xr = (const char*)x + row * 1280 + (lane & 7) * 16;
        const char* gr = (const char*)gw + (lane & 7) * 16;
        float s = 0.f;
#pragma unroll
        for (int t = 0; t < 10; ++t) {
          u32x4 a = *(const u32x4*)(xr + t * 128), c = *(const u32x4*)(gr + t * 128);
#pragma unroll
          for (int e = 0; e < 4; ++e) {
            const unsigned ua = a[e], uc = c[e];
            s = __builtin_amdgcn_fdot2_f32_bf16(__builtin_bit_cast(bf16x2_t, ua), __builtin_bit_cast(bf16x2_t, uc), s, false);
          }
        }
        s += __shfl_xor(s, 1); s += __shfl_xor(s, 2); s += __shfl_xor(s, 4);
        if ((lane & 7) == 0) __hip_atomic_store(gate + row, 1.f / (1.f + __expf(-s)), RLX_AGENT);
      }
    }
  } else if (is_mlp) {
    const int f0 = ps * 32;
    if (np == 7) p1_mlp<7>(smb, w, lane, f0, rt, b1, ws);
    else p1_mlp<6>(smb, w, lane, f0, rt, b1, ws);
  } else {
    if (w < 2) p1_qkv<3>(smb, w, lane, cg & 1);
    else p1_qkv<2>(smb, w, lane, cg & 1);
  }
  GAS u64* myflag = flags + ((is_mlp * 16 + rt) * 8 + (lane & 7)) * 16 + cg;
  GAS u64* fq = flags + (((lane & 8) ? (m0 + nm - 1) >> 2 : m0 >> 2) * 8 + (((b >> 3) ^ (b << 2)) & (NREP - 1))) * 16 + (lane & 7);
  GAS u64* f67 = fq + 2048 - (lane & 7) + 6 + (lane & 1);
  if (!is_mlp) {
    BARRIER();
    u64 fm = 0;
    p1_attn(smb, w, lane, rt, cg, ws, f67, fm);
    if (w == 4) {
      asm volatile("s_waitcnt vmcnt(3)" : "+v"(fm) :: "memory");
      if (!__all(fm == tag)) spin_flags(f67, tag);
    }
    BARRIER();
  } else {
    WAIT_VM(0);
    BARRIER_RAW();
    if (w == 4) {
      if (lane < NREP) __hip_atomic_store(myflag, tag, RLX_AGENT);
      spin_flags(f67, tag);
    }
    BARRIER_RAW();
  }
  if (w >= 4) {
    if (is_mlp) p2_loader<3>(smb, w - 4, lane, m0, s0, wl, wsp, wo, fq, tag, myflag);
    else p2_loader<2>(smb, w - 4, lane, m0, s0, wl, wsp, wo, fq, tag, myflag);
    asm volatile("" : "+v"(td));
    BARRIER_RAW();
  } else if (is_mlp) p2_compute<3>(smb, w, lane, m0, gate, (char*)out + (size_t)m0 * 32 * 1280 + ct * 128);
  else p2_compute<2>(smb, w, lane, m0, gate, (char*)out + (size_t)m0 * 32 * 1280 + ct * 128);
  if (b == 246 && w == 7) {
#pragma unroll 1
    for (int spin = 0; spin < SPIN_CAP; ++spin) {
      bool ok = true;
#pragma unroll
      for (int k = 0; k < 4; ++k) ok &= __hip_atomic_load(flags + (k * 8 + (lane >> 3)) * 128 + (lane & 7), RLX_AGENT) == tag;
      if (__all(ok)) break;
      __builtin_amdgcn_s_sleep(8);
    }
    if (lane == 0) __hip_atomic_store(epoch, tag, RLX_AGENT);
  }
}

extern "C" void kernel_launch(void* const* d_in, const int* in_sizes, int n_in, void* d_out, int out_size, void* d_ws,
                              size_t ws_size, hipStream_t stream) {
  (void)hipFuncSetAttribute((const void*)fused_kernel, hipFuncAttributeMaxDynamicSharedMemorySize, LDS_BYTES);
  fused_kernel<<<256, NTHREADS, LDS_BYTES, stream>>>((const u16*)d_in[0], (const u16*)d_in[1], (const u16*)d_in[2], (const u16*)d_in[4],
                                                     (const u16*)d_in[5], (const u16*)d_in[6], (const u16*)d_in[7], (char*)d_ws, (u16*)d_out);
}
```
